```python
import torch

def _softmax(x, axis=-1):
    return torch.softmax(x, dim=axis)
def _gelu(x):
    return torch.nn.functional.gelu(x, approximate="tanh")
def _silu(x):
    return torch.nn.functional.silu(x)
def _sigmoid(x):
    return torch.sigmoid(x)
def _layernorm(x, axis=-1, eps=1e-5):
    m = x.mean(dim=axis, keepdim=True)
    v = x.var(dim=axis, keepdim=True, unbiased=False)
    return (x - m) / torch.sqrt(v + eps)
def _scatter_add(tgt, idx, src, axis=0):
    return tgt.clone().index_add_(axis, idx, src)
def _segment_sum(src, seg, n):
    out = torch.zeros((n,) + src.shape[1:], dtype=src.dtype)
    return out.index_add_(0, seg, src)
def _pad(x, pw):
    flat = tuple(v for pair in reversed(pw) for v in pair)
    return torch.nn.functional.pad(x, flat)

kernel_name = 'recurrent_scan__sliding_block_cached__mixer_channel_mlp__decode'

def setup_inputs():

    _w = (lambda a: (lambda t: t.to(torch.bfloat16) if t.is_floating_point() else t)(torch.as_tensor(a)))
    def _A_setup():
        B, Sq, D = 4, 1, 512
        Hq, Hkv, Sk, Dh = 32, 8, 8192, 128
        assert Hq % Hkv == 0, (Hq, Hkv)
        return {
            "x": _w((lambda *s: torch.randn(*s, dtype=torch.bfloat16) * 0.05)(B, Sq, D)),
            "k_cache": _w((lambda *s: torch.randn(*s, dtype=torch.bfloat16) * 3.2)(B, Hkv, Sk, Dh)),
            "v_cache": _w((lambda *s: torch.randn(*s, dtype=torch.bfloat16) * 3.2)(B, Hkv, Sk, Dh)),
            "wq": _w((lambda *s: torch.randn(*s, dtype=torch.bfloat16) * 3.2)(D, Hq * Dh) * (D ** -0.5)),
            "wo": _w((lambda *s: torch.randn(*s, dtype=torch.bfloat16) * 3.2)(Hq * Dh, D) * ((Hq * Dh) ** -0.5)),
        }
    _A_in = _A_setup()

    def _B_setup():
        B, S, D, F = 4, 1, 512, 1792
        return {
            "x": _w((lambda *s: torch.randn(*s, dtype=torch.bfloat16) * 0.05)(B, S, D)),
            "w1": _w((lambda *s: torch.randn(*s, dtype=torch.bfloat16) * 3.2)(D, F) * (D ** -0.5)),
            "b1": _w((lambda *s: torch.randn(*s, dtype=torch.bfloat16) * 3.2)(F)),
            "w2": _w((lambda *s: torch.randn(*s, dtype=torch.bfloat16) * 3.2)(F, D) * (F ** -0.5)),
            "b2": _w((lambda *s: torch.randn(*s, dtype=torch.bfloat16) * 3.2)(D)),
        }
    _B_in = _B_setup()

    return {
        "A_x": _A_in["x"],
        "A_k_cache": _A_in["k_cache"],
        "A_v_cache": _A_in["v_cache"],
        "A_wq": _A_in["wq"],
        "A_wo": _A_in["wo"],
        "B_x": _B_in["x"],
        "B_w1": _B_in["w1"],
        "B_b1": _B_in["b1"],
        "B_w2": _B_in["w2"],
        "B_b2": _B_in["b2"],
    }

def reference(A_x, A_k_cache, A_v_cache, A_wq, A_wo, B_x, B_w1, B_b1, B_w2, B_b2):
    def _A_ref(x, k_cache, v_cache, wq, wo):
        B, Sq, D = x.shape
        Hq, Dh = 32, 128
        Hkv, Sk = k_cache.shape[1], k_cache.shape[2]
        W = min(4096, max(256, Sk // 4))
        rep = Hq // Hkv
        q = (lambda x, *d: x.permute(*d))((x @ wq).reshape(B, Sq, Hq, Dh), 0, 2, 1, 3)
        k = (lambda x, n, axis: torch.repeat_interleave(x, n, dim=axis))(k_cache[:, :, Sk - W:, :], rep, axis=1)
        v = (lambda x, n, axis: torch.repeat_interleave(x, n, dim=axis))(v_cache[:, :, Sk - W:, :], rep, axis=1)
        scores = torch.einsum("bhqd,bhkd->bhqk", q, k) * (Dh ** -0.5)
        p = _softmax(scores, axis=-1)
        o = torch.einsum("bhqk,bhkd->bhqd", p, v)
        return (lambda x, *d: x.permute(*d))(o, 0, 2, 1, 3).reshape(B, Sq, Hq * Dh) @ wo

    def _B_ref(x, w1, b1, w2, b2):
        y = _layernorm(x, axis=-1)
        h = _gelu(torch.einsum("bsd,df->bsf", y, w1) + b1)
        return torch.einsum("bsf,fd->bsd", h, w2) + b2

    x = A_x
    xn = _layernorm(x, axis=-1)
    s = _A_ref(xn, A_k_cache, A_v_cache, A_wq, A_wo)
    g = _silu(_B_ref(xn, B_w1, B_b1, B_w2, B_b2))
    return x + s * g
```

```cpp
#include <hip/hip_runtime.h>
#include <hip/hip_bf16.h>
#include <stdint.h>

#define MIMIC 0

typedef unsigned short u16;
__device__ __forceinline__ float bf2f(u16 b) { return __builtin_bit_cast(float, (unsigned)b << 16); }
__device__ __forceinline__ u16 f2bf(float f) { __hip_bfloat16 h = __float2bfloat16(f); return __builtin_bit_cast(u16, h); }
__device__ __forceinline__ float rbf(float f) {
  return f;
}

constexpr int B = 4, D = 512, HQ = 32, HKV = 8, SK = 8192, DH = 128, W = 2048, F = 1792, NS = 8;

__device__ float block_sum(float v, float* red) {
  for (int o = 32; o > 0; o >>= 1) v += __shfl_xor(v, o);
  __syncthreads();
  if ((threadIdx.x & 63) == 0) red[threadIdx.x >> 6] = v;
  __syncthreads();
  float s = 0;
  for (int i = 0; i < (int)(blockDim.x >> 6); ++i) s += red[i];
  return s;
}

__device__ void ln512(float& a, float& b, float* red) {
  float m = block_sum(a + b, red) * (1.f / 512.f);
  float v = block_sum((a - m) * (a - m) + (b - m) * (b - m), red) * (1.f / 512.f);
  float mr = rbf(m), vr = rbf(v);
  float den = rbf(sqrtf(rbf(vr + 1e-5f)));
  a = rbf(rbf(a - mr) / den);
  b = rbf(rbf(b - mr) / den);
}

__global__ void k1(const u16* x, const u16* wq, const u16* w1, const u16* b1, float* q, float* h) {
  __shared__ float xs[4][512];
  __shared__ float red[4];
  __shared__ float part[4][4][64];
  const int tid = threadIdx.x;
  const bool isq = blockIdx.x < 64;
  for (int b = 0; b < 4; ++b) {
    float a0 = bf2f(x[b * 512 + tid]), a1 = bf2f(x[b * 512 + 256 + tid]);
    ln512(a0, a1, red);
    if (!isq) ln512(a0, a1, red);
    xs[b][tid] = a0; xs[b][256 + tid] = a1;
  }
  __syncthreads();
  const int c = tid & 63, kq = tid >> 6;
  const u16* w = isq ? wq : w1;
  const int N = isq ? 4096 : F;
  const int col = (isq ? blockIdx.x : blockIdx.x - 64) * 64 + c;
  float acc[4] = {0, 0, 0, 0};
  for (int k = kq * 128; k < kq * 128 + 128; ++k) {
    float wv = bf2f(w[(size_t)k * N + col]);
    for (int b = 0; b < 4; ++b) acc[b] += xs[b][k] * wv;
  }
  for (int b = 0; b < 4; ++b) part[kq][b][c] = acc[b];
  __syncthreads();
  {
    const int b = tid >> 6;
    float s = part[0][b][c] + part[1][b][c] + part[2][b][c] + part[3][b][c];
    s = rbf(s);
    if (isq) q[b * 4096 + col] = s;
    else {
      float z = rbf(s + bf2f(b1[col]));
      float t = tanhf(0.7978845608028654f * (z + 0.044715f * z * z * z));
      h[b * F + col] = rbf(0.5f * z * (1.f + t));
    }
  }
}

__global__ void k2(const u16* kc, const u16* vc, const float* q, float* pml, float* po) {
  __shared__ float qs[4][128];
  __shared__ float ps[4][256];
  __shared__ float red[4];
  const int s = blockIdx.x, kvh = blockIdx.y, b = blockIdx.z, tid = threadIdx.x;
  for (int i = tid; i < 512; i += 256) qs[i >> 7][i & 127] = q[b * 4096 + kvh * 512 + i];
  __syncthreads();
  const size_t base = ((size_t)(b * HKV + kvh) * SK + (SK - W) + s * 256) * DH;
  const u16* kr = kc + base + (size_t)tid * DH;
  float sc[4] = {0, 0, 0, 0};
  for (int d = 0; d < 128; ++d) {
    float kv = bf2f(kr[d]);
    for (int g = 0; g < 4; ++g) sc[g] += qs[g][d] * kv;
  }
  for (int g = 0; g < 4; ++g) {
    float v = rbf(rbf(sc[g]) * 0.08838834764831845f);
    float m = v;
    for (int o = 32; o > 0; o >>= 1) m = fmaxf(m, __shfl_xor(m, o));
    __syncthreads();
    if ((tid & 63) == 0) red[tid >> 6] = m;
    __syncthreads();
    m = fmaxf(fmaxf(red[0], red[1]), fmaxf(red[2], red[3]));
    float p = __expf(v - m);
    ps[g][tid] = p;
    float l = block_sum(p, red);
    if (tid == 0) {
      const int row = (b * HQ + kvh * 4 + g) * NS + s;
      pml[row * 2] = m; pml[row * 2 + 1] = l;
    }
  }
  __syncthreads();
  const int d = tid & 127, gh = tid >> 7;
  float a0 = 0, a1 = 0;
  for (int j = 0; j < 256; ++j) {
    float vv = bf2f(vc[base + (size_t)j * DH + d]);
    a0 += ps[gh * 2][j] * vv; a1 += ps[gh * 2 + 1][j] * vv;
  }
  po[((size_t)(b * HQ + kvh * 4 + gh * 2) * NS + s) * DH + d] = a0;
  po[((size_t)(b * HQ + kvh * 4 + gh * 2 + 1) * NS + s) * DH + d] = a1;
}

__global__ void k3(const float* pml, const float* po, const float* h, const u16* wo, const u16* w2, float* part) {
  __shared__ float os[4][128];
  const int tid = threadIdx.x, blk = blockIdx.x;
  const bool iso = blk < 32;
  {
    const int b = tid >> 7, d = tid & 127;
    if (iso) {
      const int row = (b * HQ + blk) * NS;
      float M = -INFINITY;
      for (int s = 0; s < NS; ++s) M = fmaxf(M, pml[(row + s) * 2]);
      float L = 0, o = 0;
      for (int s = 0; s < NS; ++s) {
        float w = __expf(pml[(row + s) * 2] - M);
        L += w * pml[(row + s) * 2 + 1];
        o += w * po[(size_t)(row + s) * DH + d];
      }
      os[b][d] = rbf(o / L);
    } else {
      os[b][d] = h[b * F + (blk - 32) * 128 + d];
    }
  }
  __syncthreads();
  const u16* w = iso ? wo + (size_t)blk * 128 * 512 : w2 + (size_t)(blk - 32) * 128 * 512;
  float acc[4] = {0, 0, 0, 0};
  for (int k = 0; k < 128; ++k) {
    float wv = bf2f(w[k * 512 + tid]);
    for (int b = 0; b < 4; ++b) acc[b] += os[b][k] * wv;
  }
  for (int b = 0; b < 4; ++b) part[(size_t)blk * 2048 + b * 512 + tid] = acc[b];
}

__global__ void k4(const float* part, const u16* x, const u16* b2, u16* out) {
  const int i = blockIdx.x * blockDim.x + threadIdx.x;
  float s = 0, g = 0;
  for (int k = 0; k < 32; ++k) s += part[k * 2048 + i];
  for (int k = 32; k < 46; ++k) g += part[k * 2048 + i];
  s = rbf(s);
  g = rbf(rbf(g) + bf2f(b2[i & 511]));
  g = rbf(g / (1.f + __expf(-g)));
  out[i] = f2bf(bf2f(x[i]) + rbf(s * g));
}

extern "C" void kernel_launch(void* const* d_in, const int* in_sizes, int n_in,
                              void* d_out, int out_size, void* d_ws, size_t ws_size,
                              hipStream_t stream) {
  const u16* x = (const u16*)d_in[0];
  const u16* kc = (const u16*)d_in[1];
  const u16* vc = (const u16*)d_in[2];
  const u16* wq = (const u16*)d_in[3];
  const u16* wo = (const u16*)d_in[4];
  const u16* w1 = (const u16*)d_in[6];
  const u16* b1 = (const u16*)d_in[7];
  const u16* w2 = (const u16*)d_in[8];
  const u16* b2 = (const u16*)d_in[9];
  float* ws = (float*)d_ws;
  float* q = ws;
  float* h = ws + 16384;
  float* pml = ws + 32768;
  float* po = ws + 65536;
  float* part = ws + 262144;
  k1<<<92, 256, 0, stream>>>(x, wq, w1, b1, q, h);
  k2<<<dim3(NS, HKV, B), 256, 0, stream>>>(kc, vc, q, pml, po);
  k3<<<46, 512, 0, stream>>>(pml, po, h, wo, w2, part);
  k4<<<8, 256, 0, stream>>>(part, x, b2, (u16*)d_out);
}
```

```cpp
#include <hip/hip_runtime.h>
#include <stdint.h>

#define GAS __attribute__((address_space(1)))
#define LAS __attribute__((address_space(3)))
typedef unsigned short u16;
typedef unsigned long long u64;
typedef unsigned v4u __attribute__((ext_vector_type(4)));
typedef unsigned v2u __attribute__((ext_vector_type(2)));
typedef short v4s __attribute__((ext_vector_type(4)));
typedef float f32x4 __attribute__((ext_vector_type(4)));
typedef float f32x2 __attribute__((ext_vector_type(2)));
typedef __bf16 v8bf __attribute__((ext_vector_type(8)));
typedef __bf16 v2bf __attribute__((ext_vector_type(2)));
typedef __amdgpu_buffer_rsrc_t rsrc_t;

constexpr int AUX_NT = 2, AUX_SC1 = 16;
constexpr unsigned SPIN_CAP = 1u << 20;
constexpr int OOB = 0x7ffffff0;
constexpr float LOG2E = 1.4426950408889634f;

#define EXP 0
#define QSL 8
#define QAT 15
#define VWAITK 1
#define FSL 30
#define KDELAY 6
#define GSL 20
#define VPRIO 3
#define LSL 16
#define PSL 12
#define VRAMP 5
#define ROT 9
#define SKEW 2
#define KPRIO 2
#define GPS 4
#define FPS 3
#define GMODE 1
#define WK 6
#define WV 6
constexpr int EP_OFF = 0;
constexpr int QP_OFF = 4096;
constexpr int ZP_OFF = QP_OFF + 524288;
constexpr int L1_OFF = ZP_OFF + 229376;
constexpr int P1_OFF = L1_OFF + 131072;
constexpr int P2_OFF = P1_OFF + 524288;
constexpr int G2_OFF = P2_OFF + 1048576;
constexpr int WS_BYTES = G2_OFF + 1048576;
constexpr int ST_OFF = 8 << 20;

constexpr int XS_STRIDE = 1024 + 64;
constexpr int WT_RS = 160;
constexpr int L_WQ = 0;
constexpr int L_W1 = L_WQ + 128 * WT_RS;
constexpr int L_XN = L_W1 + 128 * WT_RS;
constexpr int L_YN = L_XN + 4 * XS_STRIDE;
constexpr int L_Q = L_YN + 4 * XS_STRIDE;
constexpr int L_ST = L_Q + 1024;
constexpr int L_FLAG = L_ST + 128;
constexpr int F_A = L_FLAG, F_B = L_FLAG + 16, F_Q = L_FLAG + 32, F_K = L_FLAG + 48, F_P = L_FLAG + 64, F_O = L_FLAG + 80, F_WO = L_FLAG + 96, F_OS = L_FLAG + 112, F_G = L_FLAG + 128, F_H = L_FLAG + 160;
constexpr int L_P = L_FLAG + 176;
constexpr int L_PART = L_P + 2048;
constexpr int WO_RS = 288;
constexpr int L_WO = L_PART + 8192;
constexpr int OS_STRIDE = 128 + 16;
constexpr int L_OS = L_WO + 96 * WO_RS;
constexpr int L_H = L_OS + 4 * OS_STRIDE;
constexpr int LDS_BYTES = L_H + 256;

__device__ __forceinline__ unsigned pk(float lo, float hi) { f32x2 v = {lo, hi}; return __builtin_bit_cast(unsigned, __builtin_convertvector(v, v2bf)); }
__device__ __forceinline__ float lo16(unsigned u) { return __builtin_bit_cast(float, u << 16); }
__device__ __forceinline__ float hi16(unsigned u) { return __builtin_bit_cast(float, u & 0xffff0000u); }
__device__ __forceinline__ float rbf(float x) { return lo16(pk(x, x)); }
__device__ __forceinline__ float rcp(float x) { return __builtin_amdgcn_rcpf(x); }
__device__ __forceinline__ float ex2(float x) { return __builtin_amdgcn_exp2f(x); }
template <int CTRL> __device__ __forceinline__ float dpp(float x) {
  return __builtin_bit_cast(float, __builtin_amdgcn_mov_dpp(__builtin_bit_cast(int, x), CTRL, 0xf, 0xf, true));
}
__device__ __forceinline__ float row16_sum(float x) { x += dpp<0x128>(x); x += dpp<0x124>(x); x += dpp<0x122>(x); x += dpp<0x121>(x); return x; }
__device__ __forceinline__ float oct_sum(float x) { x += dpp<0xB1>(x); x += dpp<0x4E>(x); x += dpp<0x141>(x); return x; }
__device__ __forceinline__ float oct_max(float x) { x = fmaxf(x, dpp<0xB1>(x)); x = fmaxf(x, dpp<0x4E>(x)); x = fmaxf(x, dpp<0x141>(x)); return x; }
__device__ __forceinline__ float xrow_max(float x) {
  auto s = __builtin_amdgcn_permlane16_swap(__float_as_uint(x), __float_as_uint(x), false, false);
  x = fmaxf(__uint_as_float(s[0]), __uint_as_float(s[1]));
  auto t = __builtin_amdgcn_permlane32_swap(__float_as_uint(x), __float_as_uint(x), false, false);
  return fmaxf(__uint_as_float(t[0]), __uint_as_float(t[1]));
}
__device__ __forceinline__ float xrow_sum(float x) {
  auto s = __builtin_amdgcn_permlane16_swap(__float_as_uint(x), __float_as_uint(x), false, false);
  x = __uint_as_float(s[0]) + __uint_as_float(s[1]);
  auto t = __builtin_amdgcn_permlane32_swap(__float_as_uint(x), __float_as_uint(x), false, false);
  return __uint_as_float(t[0]) + __uint_as_float(t[1]);
}
__device__ __forceinline__ f32x4 mfma(v4u a, v4u b, f32x4 c) {
  return __builtin_amdgcn_mfma_f32_16x16x32_bf16(__builtin_bit_cast(v8bf, a), __builtin_bit_cast(v8bf, b), c, 0, 0, 0);
}
__device__ __forceinline__ rsrc_t mkrs(const void* p, int bytes) { return __builtin_amdgcn_make_buffer_rsrc((void*)p, 0, bytes, 0x00020000); }
template <int AUX> __device__ __forceinline__ v4u ld128(rsrc_t r, int voff, int soff) { return __builtin_amdgcn_raw_buffer_load_b128(r, voff, soff, AUX); }
template <int AUX> __device__ __forceinline__ v2u ld64(rsrc_t r, int voff, int soff) { return __builtin_amdgcn_raw_buffer_load_b64(r, voff, soff, AUX); }
template <int AUX> __device__ __forceinline__ unsigned ld32(rsrc_t r, int voff, int soff) { return __builtin_amdgcn_raw_buffer_load_b32(r, voff, soff, AUX); }
__device__ __forceinline__ void st128(rsrc_t r, int voff, v4u v) { __builtin_amdgcn_raw_buffer_store_b128(v, r, voff, 0, AUX_SC1); }
__device__ __forceinline__ void st64(rsrc_t r, int voff, v2u v) { __builtin_amdgcn_raw_buffer_store_b64(v, r, voff, 0, AUX_SC1); }
__device__ __forceinline__ v2u trread(LAS unsigned char* p) { return __builtin_bit_cast(v2u, __builtin_amdgcn_ds_read_tr16_b64_v4i16((LAS v4s*)p)); }
#define SB() __builtin_amdgcn_sched_barrier(0)
#define CB() asm volatile("" ::: "memory")
#define BAR() do { SB(); asm volatile("s_waitcnt lgkmcnt(0)" ::: "memory"); __builtin_amdgcn_s_barrier(); asm volatile("" ::: "memory"); SB(); } while (0)
__device__ __forceinline__ int swap23(int k) { return (k & ~12) | ((k & 4) << 1) | ((k & 8) >> 1); }

#define STAMP(n) do {} while (0)

#define GL(R, K, ST) "buffer_load_dwordx4 v[" #R "+4*" #K ":" #R "+4*" #K "+3], %[o], %[rs], 0 offen offset:" #K "*" #ST " sc1\n\t"
#define GC(R, K, A, B) "v_cmp_eq_u32 %[t], %[tag], v[" #R "+4*" #K "+1]\n\ts_and_b64 vcc, vcc, %[t]\n\tv_cmp_eq_u32 %[t], %[tag], v[" #R "+4*" #K "+3]\n\ts_and_b64 vcc, vcc, %[t]\n\t" \
                       "v_mov_b32 %[" #A "], v[" #R "+4*" #K "]\n\tv_mov_b32 %[" #B "], v[" #R "+4*" #K "+2]\n\t"
#define GEND "s_andn2_b64 exec, exec, vcc\n\ts_cbranch_scc0 9f\n\t"
#define GBODY(LOAD, CHECK, NV, NH, SL) \
    "s_mov_b64 %[sv], exec\n\ts_mov_b64 exec, %[act]\n\ts_mov_b32 %[cnt], 0x100000\n" \
    "1:\n\t" \
    LOAD(208) "s_mov_b64 vcc, exec\n\ts_waitcnt vmcnt(0)\n\t" CHECK(208) GEND "s_sleep 1\n\t" \
    "s_sub_u32 %[cnt], %[cnt], 1\n\ts_cbranch_scc0 1b\n" \
    "9:\n\ts_mov_b64 exec, %[sv]\n\ts_nop 1"
#define GCLOB "vcc", "scc", "memory", "v208", "v209", "v210", "v211", "v212", "v213", "v214", "v215", "v216", "v217", "v218", "v219", "v220", "v221", "v222", "v223", \
    "v224", "v225", "v226", "v227", "v228", "v229", "v230", "v231", "v232", "v233", "v234", "v235", "v236", "v237", "v238", "v239", \
    "v240", "v241", "v242", "v243", "v244", "v245", "v246", "v247", "v248", "v249", "v250", "v251", "v252", "v253", "v254", "v255"
#define GATHER1(SL, rsd, off, tagv, actv, A0, B0) do { u64 sv_, t_; unsigned cnt_; \
    asm volatile(GBODY(G1L, G1C, 2, 1, SL) : [a0] "+v"(A0), [b0] "+v"(B0), [sv] "=&s"(sv_), [t] "=&s"(t_), [cnt] "=&s"(cnt_) \
      : [o] "v"(off), [rs] "s"(rsd), [tag] "s"(tagv), [act] "s"(actv) : GCLOB); } while (0)
#define G1L(R) GL(R, 0, 0)
#define G1C(R) GC(R, 0, a0, b0)
#define G2AL(R) GL(R, 0, 1024) GL(R, 1, 1024)
#define G2BL(R) GL(R, 0, 16) GL(R, 1, 16)
#define G2DL(R) GL(R, 0, 128) GL(R, 1, 128)
#define G2C(R) GC(R, 0, a0, b0) GC(R, 1, a1, b1)
#define G4AL(R) GL(R, 0, 1024) GL(R, 1, 1024) GL(R, 2, 1024) GL(R, 3, 1024)
#define G4BL(R) GL(R, 0, 896) GL(R, 1, 896) GL(R, 2, 896) GL(R, 3, 896)
#define G4C(R) GC(R, 0, a0, b0) GC(R, 1, a1, b1) GC(R, 2, a2, b2) GC(R, 3, a3, b3)
#define GATHER2(LOADM, SL, rsd, off, tagv, actv, D) do { u64 sv_, t_; unsigned cnt_; \
    asm volatile(GBODY(LOADM, G2C, 4, 2, SL) : [a0] "+v"(D[0]), [b0] "+v"(D[1]), [a1] "+v"(D[2]), [b1] "+v"(D[3]), [sv] "=&s"(sv_), [t] "=&s"(t_), [cnt] "=&s"(cnt_) \
      : [o] "v"(off), [rs] "s"(rsd), [tag] "s"(tagv), [act] "s"(actv) : GCLOB); } while (0)
#define GATHER4(LOADM, SL, rsd, off, tagv, actv, D) do { u64 sv_, t_; unsigned cnt_; \
    asm volatile(GBODY(LOADM, G4C, 8, 4, SL) : [a0] "+v"(D[0]), [b0] "+v"(D[1]), [a1] "+v"(D[2]), [b1] "+v"(D[3]), [a2] "+v"(D[4]), [b2] "+v"(D[5]), [a3] "+v"(D[6]), [b3] "+v"(D[7]), \
      [sv] "=&s"(sv_), [t] "=&s"(t_), [cnt] "=&s"(cnt_) \
      : [o] "v"(off), [rs] "s"(rsd), [tag] "s"(tagv), [act] "s"(actv) : GCLOB); } while (0)

#define GL2(R, K) "buffer_load_dwordx4 v[" #R "+4*" #K ":" #R "+4*" #K "+3], %[o2], %[rs], 0 offen sc1\n\t"
#define G3L(R) GL(R, 0, 128) GL(R, 1, 128) GL2(R, 2)
#define G3C(R) GC(R, 0, a0, b0) GC(R, 1, a1, b1) "v_cmp_eq_u32 %[t], %[tag], v[" #R "+9]\n\ts_and_b64 vcc, vcc, %[t]\n\tv_mov_b32 %[a2], v[" #R "+8]\n\t"
#define GATHER3(SL, rsd, off, off2, tagv, actv, D) do { u64 sv_, t_; unsigned cnt_; \
    asm volatile(GBODY(G3L, G3C, 6, 3, SL) : [a0] "+v"(D[0]), [b0] "+v"(D[1]), [a1] "+v"(D[2]), [b1] "+v"(D[3]), [a2] "+v"(D[4]), [sv] "=&s"(sv_), [t] "=&s"(t_), [cnt] "=&s"(cnt_) \
      : [o] "v"(off), [o2] "v"(off2), [rs] "s"(rsd), [tag] "s"(tagv), [act] "s"(actv) : GCLOB); } while (0)
#define GLS(R, K, OFFS, SOFF) "buffer_load_dwordx4 v[" #R "+4*" #K ":" #R "+4*" #K "+3], %[o], %[rs], " SOFF " offen offset:" #OFFS " sc1\n\t"
#define G4CL(R) GLS(R, 0, 0, "0") GLS(R, 1, 2048, "0") GLS(R, 2, 0, "%[so]") GLS(R, 3, 2048, "%[so]")
#define G4DL(R) GLS(R, 0, 0, "0") GLS(R, 1, 1792, "0") GLS(R, 2, 0, "%[so]") GLS(R, 3, 1792, "%[so]")
#define GATHER4S(LOADM, SL, rsd, off, sov, tagv, actv, D) do { u64 sv_, t_; unsigned cnt_; \
    asm volatile(GBODY(LOADM, G4C, 8, 4, SL) : [a0] "+v"(D[0]), [b0] "+v"(D[1]), [a1] "+v"(D[2]), [b1] "+v"(D[3]), [a2] "+v"(D[4]), [b2] "+v"(D[5]), [a3] "+v"(D[6]), [b3] "+v"(D[7]), \
      [sv] "=&s"(sv_), [t] "=&s"(t_), [cnt] "=&s"(cnt_) \
      : [o] "v"(off), [rs] "s"(rsd), [so] "s"(sov), [tag] "s"(tagv), [act] "s"(actv) : GCLOB); } while (0)
#define GATHER4SX(a, b, c, d, e, f, g, h) GATHER4S(a, b, c, d, e, f, g, h)
#define GATHER4X(a, b, c, d, e, f, g) GATHER4(a, b, c, d, e, f, g)
__device__ __forceinline__ v4u ln_row(v4u in) {
  float v[8];
#pragma unroll
  for (int k = 0; k < 4; ++k) { v[2 * k] = lo16(in[k]); v[2 * k + 1] = hi16(in[k]); }
  const float m = xrow_sum(row16_sum(((v[0] + v[1]) + (v[2] + v[3])) + ((v[4] + v[5]) + (v[6] + v[7])))) * (1.f / 512.f);
  float q = 0.f;
#pragma unroll
  for (int j = 0; j < 8; ++j) { const float d = v[j] - m; q = fmaf(d, d, q); }
  const float var = xrow_sum(row16_sum(q)) * (1.f / 512.f);
  const float mr = rbf(m);
  const float den = rbf(__builtin_sqrtf(rbf(rbf(var) + 1e-5f)));
  const float inv = 1.f / den;
  v4u o;
#pragma unroll
  for (int k = 0; k < 4; ++k) {
    const unsigned d = pk(v[2 * k] - mr, v[2 * k + 1] - mr);
    o[k] = pk(lo16(d) * inv, hi16(d) * inv);
  }
  return o;
}

__device__ __forceinline__ void tr8(const v4u* in, v4u (&out)[8]) {
#pragma unroll
  for (int m = 0; m < 4; ++m)
#pragma unroll
    for (int jp = 0; jp < 4; ++jp) {
      out[2 * m][jp] = __builtin_amdgcn_perm(in[jp + 4][m], in[jp][m], 0x05040100u);
      out[2 * m + 1][jp] = __builtin_amdgcn_perm(in[jp + 4][m], in[jp][m], 0x07060302u);
    }
}

template <int SLEEP = 0> __device__ __forceinline__ void lds_wait4(LAS unsigned char* p) {
  for (unsigned spins = 0; spins < (SPIN_CAP << 4); ++spins) {
    const v4u f = *(volatile LAS v4u*)p;
    if (__builtin_amdgcn_readfirstlane(f.x & f.y & f.z & f.w) != 0) break;
    if (SLEEP) __builtin_amdgcn_s_sleep(SLEEP);
  }
  CB();
}

#define SETFLAG(F) do { CB(); *(volatile LAS unsigned*)(L + (F) + w * 4) = 1u; } while (0)

__global__ void __launch_bounds__(512, 2)
fused_kernel(const u16* __restrict__ x, const u16* __restrict__ kc, const u16* __restrict__ vc, const u16* __restrict__ wq,
             const u16* __restrict__ wo, const u16* __restrict__ w1, const u16* __restrict__ b1, const u16* __restrict__ w2,
             const u16* __restrict__ b2, u16* __restrict__ out, unsigned char* __restrict__ ws) {
  __shared__ __attribute__((aligned(16))) unsigned char smem[LDS_BYTES];
  LAS unsigned char* L = (LAS unsigned char*)smem;
  STAMP(0);
  const int tid = threadIdx.x, lane = tid & 63, wave = __builtin_amdgcn_readfirstlane(tid >> 6);
  const int wg = blockIdx.x, kvh = wg & 7, r = wg >> 3, b = r >> 3, s = r & 7;
  const int l16 = lane & 15, c = lane >> 4;
  const int ks = wg >> 2, ns = wg & 3;
  if (tid < 44) *(LAS unsigned*)(L + L_FLAG + tid * 4) = 0u;
  unsigned pf = 0;
  if (wave == 3) {
    u64 pc; unsigned len;
    asm volatile("s_getpc_b64 %0\n.Lpc_here:\n\ts_mov_b32 %1, .Lk_end-.Lpc_here" : "=s"(pc), "=s"(len));
    pf = ld32<0>(mkrs((const void*)pc, len), r * 512 + lane * 8, 0);
  }
  asm volatile("" :: "s"(x), "s"(kc), "s"(vc), "s"(wq), "s"(wo), "s"(w1), "s"(b1), "s"(w2), "s"(b2), "s"(out), "s"(ws));
  BAR();
  const rsrc_t rws = mkrs(ws, WS_BYTES);
  const unsigned ep = *(const __attribute__((address_space(4))) unsigned*)(ws + EP_OFF);
  unsigned tag;
#define MAKE_TAG() asm volatile("s_add_u32 %0, %1, 1" : "=s"(tag) : "s"(ep) : "scc")
  const int w = wave & 3;
  const size_t kvbase = ((size_t)(b * 8 + kvh) * 8192 + 6144) * 128;
  int tso[4];
#pragma unroll
  for (int t = 0; t < 4; ++t) tso[t] = (32 * t + ((4 * s + w + ROT * t) & 31)) * 4096;
  const int dup = l16 & 3;
  const int u1 = kvh * 14 + r;
  const int kq = r & 3, kq1 = u1 & 3;

  v4u xr, wv[4], uv[4], K[16];
  const rsrc_t rk = mkrs(kc + kvbase, 2048 * 256);
  const int koff = (l16 >> 2) * 256 + c * 64 + dup * 16;
  if (wave >= 4) {
    __builtin_amdgcn_s_setprio(VPRIO);
    const rsrc_t rwq = mkrs(wq + kvh * 512 + (r >> 2) * 64, (EXP & 2) ? 0 : 512 * 8192);
    const rsrc_t rw1 = mkrs(w1 + (u1 >> 2) * 64, (r < 14 && !(EXP & 1)) ? 512 * 3584 : 0);
    xr = ld128<0>(mkrs(x, 4096), w * 1024 + lane * 16, 0);
    SB();
#pragma unroll
    for (int n = 0; n < 4; ++n) wv[n] = ld128<AUX_NT>(rwq, (lane >> 3) * 8192 + (lane & 7) * 16, (kq * 128 + w * 32 + n * 8) * 8192);
    SB();
#pragma unroll
    for (int n = 0; n < 4; ++n) uv[n] = ld128<AUX_NT>(rw1, (lane >> 3) * 3584 + (lane & 7) * 16, (kq1 * 128 + w * 32 + n * 8) * 3584);
    SB();
  } else {
    __builtin_amdgcn_s_sleep(KDELAY);
#pragma unroll
    for (int n = 0; n < WK; ++n) K[n] = ld128<0>(rk, koff + (n & 3) * 1024, tso[n >> 2]);
    SB();
  }
  int wave2 = wave;
  asm volatile("" : "+s"(wave2));

  if (wave2 < 4) {
    const int qoff = QP_OFF + ((kvh * 4 + b) * 4 + w) * 4096 + lane * 16;
    v4u Q[4];
#pragma unroll
    for (int n = WK; n < 16; ++n) {
      asm volatile("s_waitcnt vmcnt(%0)" :: "n"(WK - 1) : "memory");
      K[n] = ld128<0>(rk, koff + (n & 3) * 1024, tso[n >> 2]);
      SB();
      if (n == QAT) {
#pragma unroll
        for (int k = 0; k < 4; ++k) Q[k] = ld128<AUX_SC1>(rws, qoff + k * 1024, 0);
        SB();
      }
    }
    MAKE_TAG();
    SETFLAG(F_K);
    __builtin_amdgcn_s_setprio(KPRIO);
    asm volatile("" :: "v"(pf));
    STAMP(1);
    {
      float qd[8];
      unsigned bad = 0;
#pragma unroll
      for (int k = 0; k < 4; ++k) { qd[2 * k] = __uint_as_float(Q[k].x); qd[2 * k + 1] = __uint_as_float(Q[k].z); bad |= (Q[k].y ^ tag) | (Q[k].w ^ tag); }
      const u64 miss = __builtin_amdgcn_ballot_w64(bad != 0);
      if (miss) GATHER4X(G4AL, QSL, rws, qoff, tag, miss, qd);
      *(LAS unsigned*)(L + L_Q + w * 256 + lane * 4) = pk((qd[0] + qd[2]) + (qd[4] + qd[6]), (qd[1] + qd[3]) + (qd[5] + qd[7]));
      SETFLAG(F_Q);
    }
    v4u Z[4];
    unsigned bias = 0;
    const int zoff = wg < 224 ? ZP_OFF + ks * 4096 + lane * 16 : OOB;
    if (wave2 == 3) {
#pragma unroll
      for (int k = 0; k < 4; ++k) Z[k] = ld128<AUX_SC1>(rws, zoff + k * 1024, 0);
      bias = ld32<0>(mkrs(b1, 3584), (ks * 32 + l16 * 2) * 2, 0);
      SB();
    }
    STAMP(2);
    lds_wait4(L + F_Q);
    STAMP(13);
    const v4u qf = *(LAS v4u*)(L + L_Q + dup * 256 + c * 64 + (l16 >> 2) * 16);
    float sc[4];
#pragma unroll
    for (int mm = 0; mm < 4; ++mm) {
      float t[4];
#pragma unroll
      for (int be = 0; be < 4; ++be) {
        const f32x4 d = mfma(K[4 * mm + be], qf, f32x4{0.f, 0.f, 0.f, 0.f});
        t[be] = ((d[be] + dpp<0x12C>(d[(be + 1) & 3])) + dpp<0x128>(d[(be + 2) & 3])) + dpp<0x124>(d[(be + 3) & 3]);
      }
      const float t01 = (l16 & 4) ? t[1] : t[0], t23 = (l16 & 4) ? t[3] : t[2];
      sc[mm] = (l16 & 8) ? t23 : t01;
    }
    {
      const unsigned d0 = pk(sc[0], sc[1]), d1 = pk(sc[2], sc[3]);
      const unsigned e0 = pk(lo16(d0) * 0.08838834764831845f, hi16(d0) * 0.08838834764831845f);
      const unsigned e1 = pk(lo16(d1) * 0.08838834764831845f, hi16(d1) * 0.08838834764831845f);
      sc[0] = lo16(e0); sc[1] = hi16(e0); sc[2] = lo16(e1); sc[3] = hi16(e1);
    }
    float m = fmaxf(fmaxf(sc[0], sc[1]), fmaxf(sc[2], sc[3]));
    m = fmaxf(m, dpp<0x124>(m)); m = fmaxf(m, dpp<0x128>(m));
    m = xrow_max(m);
    const float p0 = ex2((sc[0] - m) * LOG2E), p1 = ex2((sc[1] - m) * LOG2E), p2 = ex2((sc[2] - m) * LOG2E), p3 = ex2((sc[3] - m) * LOG2E);
    *(LAS unsigned*)(L + L_P + w * 512 + dup * 64 + c * 16 + (l16 >> 2) * 4) = pk(p0, p1);
    *(LAS unsigned*)(L + L_P + w * 512 + 256 + dup * 64 + c * 16 + (l16 >> 2) * 4) = pk(p2, p3);
    float l = (p0 + p1) + (p2 + p3);
    l += dpp<0x124>(l); l += dpp<0x128>(l);
    l = xrow_sum(l);
    if (lane < 4) *(LAS f32x2*)(L + L_ST + lane * 32 + w * 8) = f32x2{m, l};
    SETFLAG(F_P);
    STAMP(3);
    if (wave2 == 3) {
      float zd[8];
      unsigned bad = 0;
#pragma unroll
      for (int k = 0; k < 4; ++k) { zd[2 * k] = __uint_as_float(Z[k].x); zd[2 * k + 1] = __uint_as_float(Z[k].z); bad |= (Z[k].y ^ tag) | (Z[k].w ^ tag); }
      const u64 miss = __builtin_amdgcn_ballot_w64(bad != 0);
      if (wg < 224 && miss) GATHER4(G4AL, 8, rws, zoff, tag, miss, zd);
      const unsigned zp = pk((zd[0] + zd[2]) + (zd[4] + zd[6]), (zd[1] + zd[3]) + (zd[5] + zd[7]));
      const unsigned zb = pk(lo16(zp) + lo16(bias), hi16(zp) + hi16(bias));
      float z[2] = {lo16(zb), hi16(zb)};
#pragma unroll
      for (int i = 0; i < 2; ++i) {
        const float zz = z[i];
        const float u = 0.7978845608028654f * (zz + 0.044715f * zz * zz * zz);
        const float th = 1.f - 2.f * rcp(ex2(u * (2.f * LOG2E)) + 1.f);
        z[i] = 0.5f * zz * (1.f + th);
      }
      *(LAS unsigned*)(L + L_H + lane * 4) = pk(z[0], z[1]);
      CB();
      if (lane < 4) *(volatile LAS unsigned*)(L + F_H + lane * 4) = 1u;
    }
    STAMP(4);
    const int oidx = (wg >> 6) * 512 + (wg & 7) * 64 + ((wg >> 5) & 1) * 32 + ((wg >> 3) & 3) * 4 + ((lane >> 1) & 1) * 16 + (lane & 1) * 2;
    const unsigned xw = ld32<0>(mkrs(x, 4096), oidx * 2, 0);
    const unsigned bw = ld32<0>(mkrs(b2, 1024), (oidx & 511) * 2, 0);
    const int sp = lane & 7, k4 = lane >> 3;
    const int osw = L_OS + w * OS_STRIDE + k4 * 8;
    const int p1g = P1_OFF + (ks * 32 + ((w * 8 + sp + SKEW * ks) & 31)) * 256 + k4 * 16;
    const int l1g = L1_OFF + (((ks >> 1) * 4 + w) * 8 + sp) * 128;
    asm volatile("" :: "v"(osw), "v"(l1g), "v"(p1g), "v"(xw), "v"(bw));
    lds_wait4<2>(L + F_O);
    __builtin_amdgcn_s_sleep(PSL);
    STAMP(12);
    unsigned d[5] = {0u, 0u, 0u, 0u, 0u};
    GATHER3(4, rws, p1g, l1g, tag, ~0ull, d);
    STAMP(6);
    {
      const float lse = __uint_as_float(d[4]);
      const float e = ex2(lse - oct_max(lse));
      const float inv = rcp(oct_sum(e));
      float a[8];
#pragma unroll
      for (int k = 0; k < 4; ++k) { a[2 * k] = oct_sum(e * lo16(d[k])) * inv; a[2 * k + 1] = oct_sum(e * hi16(d[k])) * inv; }
      if (sp == 0) {
        *(LAS v2u*)(L + osw) = v2u{pk(a[0], a[1]), pk(a[2], a[3])};
        *(LAS v2u*)(L + osw + 64) = v2u{pk(a[4], a[5]), pk(a[6], a[7])};
      }
      SETFLAG(F_OS);
    }
    const int foff = ((wg >> 7) * 64 + (wg & 63)) * 8192 + ((wg >> 6) & 1) * 64 + (lane >> 2) * 128 + (lane & 3) * 16;
    if (wave2 == 1) {
      float dg[8] = {0.f, 0.f, 0.f, 0.f, 0.f, 0.f, 0.f, 0.f};
      GATHER4SX(G4DL, GPS, rws, G2_OFF + foff, 3584, tag, (1ull << 56) - 1ull, dg);
      float g0 = (dg[0] + dg[2]) + (dg[4] + dg[6]);
      float g1 = (dg[1] + dg[3]) + (dg[5] + dg[7]);
      g0 += dpp<0x124>(g0); g1 += dpp<0x124>(g1);
      g0 += dpp<0x128>(g0); g1 += dpp<0x128>(g1);
      g0 = xrow_sum(g0); g1 = xrow_sum(g1);
      const unsigned gp = pk(g0, g1);
      const unsigned gb = pk(lo16(gp) + lo16(bw), hi16(gp) + hi16(bw));
      const float z0 = lo16(gb), z1 = hi16(gb);
      const unsigned sg = pk(z0 * rcp(1.f + ex2(-z0 * LOG2E)), z1 * rcp(1.f + ex2(-z1 * LOG2E)));
      if (lane < 4) *(volatile LAS v2u*)(L + F_G + lane * 8) = v2u{sg, 1u};
      STAMP(8);
    } else if (wave2 == 0) {
      __builtin_amdgcn_s_sleep(FSL);
      float ds[8] = {0.f, 0.f, 0.f, 0.f, 0.f, 0.f, 0.f, 0.f};
      GATHER4SX(G4CL, FPS, rws, P2_OFF + foff, 4096, tag, ~0ull, ds);
      STAMP(9);
      st128(rws, (wg == 0 && lane < 4) ? EP_OFF + lane * 16 : OOB, v4u{tag, tag, tag, tag});
      v2u sgt = *(volatile LAS v2u*)(L + F_G + (lane & 3) * 8);
      float s0 = (ds[0] + ds[2]) + (ds[4] + ds[6]);
      float s1 = (ds[1] + ds[3]) + (ds[5] + ds[7]);
      s0 += dpp<0x124>(s0); s1 += dpp<0x124>(s1);
      s0 += dpp<0x128>(s0); s1 += dpp<0x128>(s1);
      s0 = xrow_sum(s0); s1 = xrow_sum(s1);
      const unsigned sq = pk(s0, s1);
      for (unsigned spins = 0; !__all(sgt.y != 0u) && spins < (SPIN_CAP << 4); ++spins) sgt = *(volatile LAS v2u*)(L + F_G + (lane & 3) * 8);
      const unsigned pr = pk(lo16(sq) * lo16(sgt.x), hi16(sq) * hi16(sgt.x));
      const unsigned res = pk(lo16(xw) + lo16(pr), hi16(xw) + hi16(pr));
      if (lane < 4) *(GAS unsigned*)(out + oidx) = res;
      STAMP(10);
    }
  } else {
    const v4u xn = ln_row(xr);
    *(LAS v4u*)(L + L_XN + w * XS_STRIDE + lane * 16) = xn;
    MAKE_TAG();
    STAMP(1);
    const int sw = (w * 32 + ((lane >> 5) & 1) * 8 + ((lane >> 3) & 3)) * WT_RS + (lane & 7) * 16;
#pragma unroll
    for (int n = 0; n < 4; ++n) *(LAS v4u*)(L + L_WQ + sw + ((n >> 1) * 16 + (n & 1) * 4) * WT_RS) = wv[n];
    SETFLAG(F_A);
    const int ta = ((c >> 1) * 16 + (c & 1) * 4 + (l16 >> 2)) * WT_RS + w * 32 + (l16 & 3) * 8;
    const int xb = (l16 >> 2) * XS_STRIDE + c * 16;
    lds_wait4(L + F_A);
    {
      f32x4 acc = {0.f, 0.f, 0.f, 0.f};
#pragma unroll
      for (int kk = 0; kk < 4; ++kk) {
        const v2u a0 = trread(L + L_WQ + ta + kk * 32 * WT_RS), a1 = trread(L + L_WQ + ta + (kk * 32 + 8) * WT_RS);
        acc = mfma(*(LAS v4u*)(L + L_XN + xb + kq * 256 + kk * 64), v4u{a0.x, a0.y, a1.x, a1.y}, acc);
      }
      st64(rws, QP_OFF + ((((kvh * 4 + c) * 4 + (r >> 3)) * 4 + kq) * 128 + ((r >> 2) & 1) * 64 + w * 16 + l16) * 8, v2u{__float_as_uint(acc[0]), tag});
    }
    SB();
    STAMP(2);
    const v4u yn = ln_row(xn);
    *(LAS v4u*)(L + L_YN + w * XS_STRIDE + lane * 16) = yn;
#pragma unroll
    for (int n = 0; n < 4; ++n) *(LAS v4u*)(L + L_W1 + sw + ((n >> 1) * 16 + (n & 1) * 4) * WT_RS) = uv[n];
    SETFLAG(F_B);
    lds_wait4(L + F_B);
    {
      f32x4 acc = {0.f, 0.f, 0.f, 0.f};
#pragma unroll
      for (int kk = 0; kk < 4; ++kk) {
        const v2u a0 = trread(L + L_W1 + ta + kk * 32 * WT_RS), a1 = trread(L + L_W1 + ta + (kk * 32 + 8) * WT_RS);
        acc = mfma(*(LAS v4u*)(L + L_YN + xb + kq1 * 256 + kk * 64), v4u{a0.x, a0.y, a1.x, a1.y}, acc);
      }
      const int off = ZP_OFF + (((((u1 >> 2) * 2 + (w >> 1)) * 4 + kq1) * 4 + c) * 32 + (w & 1) * 16 + l16) * 8;
      st64(rws, r < 14 ? off : OOB, v2u{__float_as_uint(acc[0]), tag});
    }
    __builtin_amdgcn_s_setprio(0);
    STAMP(3);
    for (unsigned spins = 0; spins < (SPIN_CAP << 4); ++spins) {
      if (*(volatile LAS unsigned*)(L + F_K + w * 4) != 0u) break;
      __builtin_amdgcn_s_sleep(1);
    }
    const rsrc_t rv = mkrs(vc + kvbase, 2048 * 256);
    const int voff = lane * 16;
    const rsrc_t rwo = mkrs(wo + (size_t)ks * 64 * 512 + ns * 128, 64 * 1024);
    const rsrc_t rw2 = mkrs(w2 + (size_t)ks * 32 * 512 + ns * 128, wg < 224 ? 32 * 1024 : 0);
    v4u V[16], WO[6];
#pragma unroll
    for (int n = 0; n < 16; ++n) {
      if (n >= WV) asm volatile("s_waitcnt vmcnt(%0)" :: "n"(WV - 1) : "memory");
      V[n] = ld128<0>(rv, voff + (n & 3) * 1024, tso[n >> 2]);
      SB();
      if (n < WV - 1 && VRAMP) { __builtin_amdgcn_s_sleep(VRAMP); SB(); }
    }
    STAMP(4);
#define WO_LOAD(n) do { asm volatile("s_waitcnt vmcnt(%0)" :: "n"(WV - 1) : "memory"); \
      WO[n] = (n) < 4 ? ld128<AUX_NT>(rwo, (16 * w + c) * 1024 + l16 * 16, (n) * 4096) : ld128<AUX_NT>(rw2, (8 * w + c) * 1024 + l16 * 16, ((n) - 4) * 4096); SB(); } while (0)
    WO_LOAD(0); WO_LOAD(1);
    lds_wait4<1>(L + F_P);
    v4u P[2];
    P[0] = *(LAS v4u*)(L + L_P + w * 512 + (l16 >> 2) * 64 + c * 16);
    P[1] = *(LAS v4u*)(L + L_P + w * 512 + 256 + (l16 >> 2) * 64 + c * 16);
    float wgt[4];
    unsigned lse2;
    {
      const f32x4 s0 = *(LAS f32x4*)(L + L_ST + w * 32), s1 = *(LAS f32x4*)(L + L_ST + w * 32 + 16);
      const float M = fmaxf(fmaxf(s0[0], s0[2]), fmaxf(s1[0], s1[2]));
      wgt[0] = ex2((s0[0] - M) * LOG2E);
      wgt[1] = ex2((s0[2] - M) * LOG2E);
      wgt[2] = ex2((s1[0] - M) * LOG2E);
      wgt[3] = ex2((s1[2] - M) * LOG2E);
      const float Ls = wgt[0] * s0[1] + wgt[1] * s0[3] + wgt[2] * s1[1] + wgt[3] * s1[3];
      const float inv = rcp(Ls);
#pragma unroll
      for (int k = 0; k < 4; ++k) wgt[k] *= inv;
      lse2 = __float_as_uint(M * LOG2E + __builtin_amdgcn_logf(Ls));
    }
    const int ksp = (kvh * 4 + w) * 2 + (c & 1);
    const int p1off = lane < 32 ? P1_OFF + (ksp * 32 + ((r + SKEW * ksp) & 31)) * 256 + l16 * 16
                    : lane < 40 ? L1_OFF + (((kvh * 4 + w) * 4 + b) * 8 + s) * 128 + l16 * 16 : OOB;
    const int ppw = L_PART + w * 2048 + c * 512 + l16 * 32, ppr = L_PART + w * 512 + (lane & 31) * 16;
    f32x4 o[8];
    {
      v4u vt[8];
      tr8(&V[0], vt);
#pragma unroll
      for (int e = 0; e < 8; ++e) o[e] = mfma(P[0], vt[e], f32x4{0.f, 0.f, 0.f, 0.f});
    }
    asm volatile("" :: "v"(p1off), "v"(ppw), "v"(ppr), "v"(wgt[0]), "v"(wgt[1]), "v"(wgt[2]), "v"(wgt[3]), "v"(lse2));
    SB();
    WO_LOAD(2); WO_LOAD(3); WO_LOAD(4); WO_LOAD(5);
    {
      v4u vt[8];
      tr8(&V[8], vt);
#pragma unroll
      for (int e = 0; e < 8; ++e) o[e] = mfma(P[1], vt[e], o[e]);
    }
    ((LAS f32x4*)(L + ppw))[0] = f32x4{o[0][0], o[1][0], o[2][0], o[3][0]};
    ((LAS f32x4*)(L + ppw))[1] = f32x4{o[4][0], o[5][0], o[6][0], o[7][0]};
    SETFLAG(F_O);
    STAMP(11);
    lds_wait4(L + F_O);
    {
      const LAS f32x4* rp = (const LAS f32x4*)(L + ppr);
      const f32x4 a = (wgt[0] * rp[0] + wgt[1] * rp[128]) + (wgt[2] * rp[256] + wgt[3] * rp[384]);
      st128(rws, p1off, v4u{lane < 32 ? pk(a[0], a[1]) : lse2, tag, pk(a[2], a[3]), tag});
    }
    STAMP(5);
#pragma unroll
    for (int n = 0; n < 4; ++n) *(LAS v4u*)(L + L_WO + swap23(16 * w + 4 * n + c) * WO_RS + l16 * 16) = WO[n];
#pragma unroll
    for (int n = 0; n < 2; ++n) *(LAS v4u*)(L + L_WO + (64 + swap23(8 * w + 4 * n + c)) * WO_RS + l16 * 16) = WO[4 + n];
    SETFLAG(F_WO);
    STAMP(14);
    const int fpair = (ns * 2 + (w >> 1)) + 8 * ((l16 >> 3) * 8 + (w & 1) * 4 + c);
    const int sk = ks + SKEW * (fpair >> 3);
    const int p2off = P2_OFF + (fpair * 64 + (sk & 63)) * 128 + (l16 & 7) * 16;
    const int g2off = G2_OFF + (fpair * 64 + (sk >= 56 ? sk - 56 : sk)) * 128 + (l16 & 7) * 16;
    const int osr = L_OS + (l16 >> 2) * OS_STRIDE + c * 16;
    lds_wait4(L + F_WO);
    v4u af[2][2];
    {
      LAS unsigned char* tw = L + L_WO + ((c >> 1) * 16 + (c & 1) * 4 + (l16 >> 2)) * WO_RS + w * 64 + (l16 & 3) * 8;
#pragma unroll
      for (int ct = 0; ct < 2; ++ct)
#pragma unroll
        for (int kk = 0; kk < 2; ++kk) {
          const v2u a0 = trread(tw + ct * 32 + kk * 32 * WO_RS), a1 = trread(tw + ct * 32 + kk * 32 * WO_RS + 8 * WO_RS);
          af[ct][kk] = v4u{a0.x, a0.y, a1.x, a1.y};
        }
      lds_wait4<1>(L + F_H);
      const v4u hb = *(LAS v4u*)(L + L_H + (l16 >> 2) * 64 + c * 16);
      f32x4 g[2];
#pragma unroll
      for (int ct = 0; ct < 2; ++ct) {
        const v2u a0 = trread(tw + ct * 32 + 64 * WO_RS), a1 = trread(tw + ct * 32 + 72 * WO_RS);
        g[ct] = mfma(v4u{a0.x, a0.y, a1.x, a1.y}, hb, f32x4{0.f, 0.f, 0.f, 0.f});
      }
      const f32x4 acc = (dup & 2) ? g[1] : g[0];
      const float v0 = (dup & 1) ? acc[2] : acc[0], v1 = (dup & 1) ? acc[3] : acc[1];
      st128(rws, wg < 224 ? g2off : OOB, v4u{__float_as_uint(v0), tag, __float_as_uint(v1), tag});
    }
    asm volatile("" :: "v"(p2off), "v"(osr), "v"(af[0][0]), "v"(af[0][1]), "v"(af[1][0]), "v"(af[1][1]));
    STAMP(6);
    lds_wait4(L + F_OS);
    {
      const v4u b0 = *(LAS v4u*)(L + osr), b1f = *(LAS v4u*)(L + osr + 64);
      f32x4 acc0 = mfma(af[0][0], b0, f32x4{0.f, 0.f, 0.f, 0.f});
      f32x4 acc1 = mfma(af[1][0], b0, f32x4{0.f, 0.f, 0.f, 0.f});
      acc0 = mfma(af[0][1], b1f, acc0);
      acc1 = mfma(af[1][1], b1f, acc1);
      const f32x4 acc = (dup & 2) ? acc1 : acc0;
      const float v0 = (dup & 1) ? acc[2] : acc[0], v1 = (dup & 1) ? acc[3] : acc[1];
      st128(rws, p2off, v4u{__float_as_uint(v0), tag, __float_as_uint(v1), tag});
    }
    STAMP(7);
  }
  asm volatile(".Lk_end:");
}

extern "C" void kernel_launch(void* const* d_in, const int* in_sizes, int n_in,
                              void* d_out, int out_size, void* d_ws, size_t ws_size,
                              hipStream_t stream) {
  fused_kernel<<<256, 512, 0, stream>>>((const u16*)d_in[0], (const u16*)d_in[1], (const u16*)d_in[2], (const u16*)d_in[3],
                                        (const u16*)d_in[4], (const u16*)d_in[6], (const u16*)d_in[7], (const u16*)d_in[8],
                                        (const u16*)d_in[9], (u16*)d_out, (unsigned char*)d_ws);
}
```

```cpp
#include <hip/hip_runtime.h>
#include <stdint.h>
#define PADW 4
#define PADN 1

#define GAS __attribute__((address_space(1)))
#define LAS __attribute__((address_space(3)))
typedef unsigned short u16;
typedef unsigned long long u64;
typedef unsigned v4u __attribute__((ext_vector_type(4)));
typedef unsigned v2u __attribute__((ext_vector_type(2)));
typedef short v4s __attribute__((ext_vector_type(4)));
typedef float f32x4 __attribute__((ext_vector_type(4)));
typedef float f32x2 __attribute__((ext_vector_type(2)));
typedef __bf16 v8bf __attribute__((ext_vector_type(8)));
typedef __bf16 v2bf __attribute__((ext_vector_type(2)));
typedef __amdgpu_buffer_rsrc_t rsrc_t;

constexpr int AUX_NT = 2, AUX_SC1 = 16;
constexpr unsigned SPIN_CAP = 1u << 20;
constexpr int OOB = 0x7ffffff0;
constexpr float LOG2E = 1.4426950408889634f;

#define EXP 0
#define QSL 8
#define QAT 15
#define VWAITK 1
#define FSL 30
#define KDELAY 6
#define GSL 20
#define VPRIO 3
#define LSL 16
#define PSL 12
#define VRAMP 5
#define ROT 9
#define SKEW 2
#define KPRIO 2
#define GPS 4
#define FPS 3
#define GMODE 1
#define WK 6
#define WV 6
constexpr int EP_OFF = 0;
constexpr int QP_OFF = 4096;
constexpr int ZP_OFF = QP_OFF + 524288;
constexpr int L1_OFF = ZP_OFF + 229376;
constexpr int P1_OFF = L1_OFF + 131072;
constexpr int P2_OFF = P1_OFF + 524288;
constexpr int G2_OFF = P2_OFF + 1048576;
constexpr int WS_BYTES = G2_OFF + 1048576;
constexpr int ST_OFF = 8 << 20;

constexpr int XS_STRIDE = 1024 + 64;
constexpr int WT_RS = 160;
constexpr int L_WQ = 0;
constexpr int L_W1 = L_WQ + 128 * WT_RS;
constexpr int L_XN = L_W1 + 128 * WT_RS;
constexpr int L_YN = L_XN + 4 * XS_STRIDE;
constexpr int L_Q = L_YN + 4 * XS_STRIDE;
constexpr int L_ST = L_Q + 1024;
constexpr int L_FLAG = L_ST + 128;
constexpr int F_A = L_FLAG, F_B = L_FLAG + 16, F_Q = L_FLAG + 32, F_K = L_FLAG + 48, F_P = L_FLAG + 64, F_O = L_FLAG + 80, F_WO = L_FLAG + 96, F_OS = L_FLAG + 112, F_G = L_FLAG + 128, F_H = L_FLAG + 160;
constexpr int L_P = L_FLAG + 176;
constexpr int L_PART = L_P + 2048;
constexpr int WO_RS = 288;
constexpr int L_WO = L_PART + 8192;
constexpr int OS_STRIDE = 128 + 16;
constexpr int L_OS = L_WO + 96 * WO_RS;
constexpr int L_H = L_OS + 4 * OS_STRIDE;
constexpr int LDS_BYTES = L_H + 256;

__device__ __forceinline__ unsigned pk(float lo, float hi) { f32x2 v = {lo, hi}; return __builtin_bit_cast(unsigned, __builtin_convertvector(v, v2bf)); }
__device__ __forceinline__ float lo16(unsigned u) { return __builtin_bit_cast(float, u << 16); }
__device__ __forceinline__ float hi16(unsigned u) { return __builtin_bit_cast(float, u & 0xffff0000u); }
__device__ __forceinline__ float rbf(float x) { return lo16(pk(x, x)); }
__device__ __forceinline__ float rcp(float x) { return __builtin_amdgcn_rcpf(x); }
__device__ __forceinline__ float ex2(float x) { return __builtin_amdgcn_exp2f(x); }
template <int CTRL> __device__ __forceinline__ float dpp(float x) {
  return __builtin_bit_cast(float, __builtin_amdgcn_mov_dpp(__builtin_bit_cast(int, x), CTRL, 0xf, 0xf, true));
}
__device__ __forceinline__ float row16_sum(float x) { x += dpp<0x128>(x); x += dpp<0x124>(x); x += dpp<0x122>(x); x += dpp<0x121>(x); return x; }
__device__ __forceinline__ float oct_sum(float x) { x += dpp<0xB1>(x); x += dpp<0x4E>(x); x += dpp<0x141>(x); return x; }
__device__ __forceinline__ float oct_max(float x) { x = fmaxf(x, dpp<0xB1>(x)); x = fmaxf(x, dpp<0x4E>(x)); x = fmaxf(x, dpp<0x141>(x)); return x; }
__device__ __forceinline__ float xrow_max(float x) {
  auto s = __builtin_amdgcn_permlane16_swap(__float_as_uint(x), __float_as_uint(x), false, false);
  x = fmaxf(__uint_as_float(s[0]), __uint_as_float(s[1]));
  auto t = __builtin_amdgcn_permlane32_swap(__float_as_uint(x), __float_as_uint(x), false, false);
  return fmaxf(__uint_as_float(t[0]), __uint_as_float(t[1]));
}
__device__ __forceinline__ float xrow_sum(float x) {
  auto s = __builtin_amdgcn_permlane16_swap(__float_as_uint(x), __float_as_uint(x), false, false);
  x = __uint_as_float(s[0]) + __uint_as_float(s[1]);
  auto t = __builtin_amdgcn_permlane32_swap(__float_as_uint(x), __float_as_uint(x), false, false);
  return __uint_as_float(t[0]) + __uint_as_float(t[1]);
}
__device__ __forceinline__ f32x4 mfma(v4u a, v4u b, f32x4 c) {
  return __builtin_amdgcn_mfma_f32_16x16x32_bf16(__builtin_bit_cast(v8bf, a), __builtin_bit_cast(v8bf, b), c, 0, 0, 0);
}
__device__ __forceinline__ rsrc_t mkrs(const void* p, int bytes) { return __builtin_amdgcn_make_buffer_rsrc((void*)p, 0, bytes, 0x00020000); }
template <int AUX> __device__ __forceinline__ v4u ld128(rsrc_t r, int voff, int soff) { return __builtin_amdgcn_raw_buffer_load_b128(r, voff, soff, AUX); }
template <int AUX> __device__ __forceinline__ v2u ld64(rsrc_t r, int voff, int soff) { return __builtin_amdgcn_raw_buffer_load_b64(r, voff, soff, AUX); }
template <int AUX> __device__ __forceinline__ unsigned ld32(rsrc_t r, int voff, int soff) { return __builtin_amdgcn_raw_buffer_load_b32(r, voff, soff, AUX); }
__device__ __forceinline__ void st128(rsrc_t r, int voff, v4u v) { __builtin_amdgcn_raw_buffer_store_b128(v, r, voff, 0, AUX_SC1); }
__device__ __forceinline__ void st64(rsrc_t r, int voff, v2u v) { __builtin_amdgcn_raw_buffer_store_b64(v, r, voff, 0, AUX_SC1); }
__device__ __forceinline__ v2u trread(LAS unsigned char* p) { return __builtin_bit_cast(v2u, __builtin_amdgcn_ds_read_tr16_b64_v4i16((LAS v4s*)p)); }
#define SB() __builtin_amdgcn_sched_barrier(0)
#define CB() asm volatile("" ::: "memory")
#define BAR() do { SB(); asm volatile("s_waitcnt lgkmcnt(0)" ::: "memory"); __builtin_amdgcn_s_barrier(); asm volatile("" ::: "memory"); SB(); } while (0)
__device__ __forceinline__ int swap23(int k) { return (k & ~12) | ((k & 4) << 1) | ((k & 8) >> 1); }

#define STAMP(n) do { if ((n) == PADN) { if (__builtin_amdgcn_readfirstlane(threadIdx.x >> 6) == PADW) { u64 t_; SB(); asm volatile("s_memrealtime %0\n\ts_waitcnt lgkmcnt(0)" : "=s"(t_) :: "memory"); SB(); *(volatile LAS u64*)(L + LDS_BYTES) = t_; } } } while (0)

#define GL(R, K, ST) "buffer_load_dwordx4 v[" #R "+4*" #K ":" #R "+4*" #K "+3], %[o], %[rs], 0 offen offset:" #K "*" #ST " sc1\n\t"
#define GC(R, K, A, B) "v_cmp_eq_u32 %[t], %[tag], v[" #R "+4*" #K "+1]\n\ts_and_b64 vcc, vcc, %[t]\n\tv_cmp_eq_u32 %[t], %[tag], v[" #R "+4*" #K "+3]\n\ts_and_b64 vcc, vcc, %[t]\n\t" \
                       "v_mov_b32 %[" #A "], v[" #R "+4*" #K "]\n\tv_mov_b32 %[" #B "], v[" #R "+4*" #K "+2]\n\t"
#define GEND "s_andn2_b64 exec, exec, vcc\n\ts_cbranch_scc0 9f\n\t"
#define GBODY(LOAD, CHECK, NV, NH, SL) \
    "s_mov_b64 %[sv], exec\n\ts_mov_b64 exec, %[act]\n\ts_mov_b32 %[cnt], 0x100000\n" \
    "1:\n\t" \
    LOAD(208) "s_mov_b64 vcc, exec\n\ts_waitcnt vmcnt(0)\n\t" CHECK(208) GEND "s_sleep 1\n\t" \
    "s_sub_u32 %[cnt], %[cnt], 1\n\ts_cbranch_scc0 1b\n" \
    "9:\n\ts_mov_b64 exec, %[sv]\n\ts_nop 1"
#define GCLOB "vcc", "scc", "memory", "v208", "v209", "v210", "v211", "v212", "v213", "v214", "v215", "v216", "v217", "v218", "v219", "v220", "v221", "v222", "v223", \
    "v224", "v225", "v226", "v227", "v228", "v229", "v230", "v231", "v232", "v233", "v234", "v235", "v236", "v237", "v238", "v239", \
    "v240", "v241", "v242", "v243", "v244", "v245", "v246", "v247", "v248", "v249", "v250", "v251", "v252", "v253", "v254", "v255"
#define GATHER1(SL, rsd, off, tagv, actv, A0, B0) do { u64 sv_, t_; unsigned cnt_; \
    asm volatile(GBODY(G1L, G1C, 2, 1, SL) : [a0] "+v"(A0), [b0] "+v"(B0), [sv] "=&s"(sv_), [t] "=&s"(t_), [cnt] "=&s"(cnt_) \
      : [o] "v"(off), [rs] "s"(rsd), [tag] "s"(tagv), [act] "s"(actv) : GCLOB); } while (0)
#define G1L(R) GL(R, 0, 0)
#define G1C(R) GC(R, 0, a0, b0)
#define G2AL(R) GL(R, 0, 1024) GL(R, 1, 1024)
#define G2BL(R) GL(R, 0, 16) GL(R, 1, 16)
#define G2DL(R) GL(R, 0, 128) GL(R, 1, 128)
#define G2C(R) GC(R, 0, a0, b0) GC(R, 1, a1, b1)
#define G4AL(R) GL(R, 0, 1024) GL(R, 1, 1024) GL(R, 2, 1024) GL(R, 3, 1024)
#define G4BL(R) GL(R, 0, 896) GL(R, 1, 896) GL(R, 2, 896) GL(R, 3, 896)
#define G4C(R) GC(R, 0, a0, b0) GC(R, 1, a1, b1) GC(R, 2, a2, b2) GC(R, 3, a3, b3)
#define GATHER2(LOADM, SL, rsd, off, tagv, actv, D) do { u64 sv_, t_; unsigned cnt_; \
    asm volatile(GBODY(LOADM, G2C, 4, 2, SL) : [a0] "+v"(D[0]), [b0] "+v"(D[1]), [a1] "+v"(D[2]), [b1] "+v"(D[3]), [sv] "=&s"(sv_), [t] "=&s"(t_), [cnt] "=&s"(cnt_) \
      : [o] "v"(off), [rs] "s"(rsd), [tag] "s"(tagv), [act] "s"(actv) : GCLOB); } while (0)
#define GATHER4(LOADM, SL, rsd, off, tagv, actv, D) do { u64 sv_, t_; unsigned cnt_; \
    asm volatile(GBODY(LOADM, G4C, 8, 4, SL) : [a0] "+v"(D[0]), [b0] "+v"(D[1]), [a1] "+v"(D[2]), [b1] "+v"(D[3]), [a2] "+v"(D[4]), [b2] "+v"(D[5]), [a3] "+v"(D[6]), [b3] "+v"(D[7]), \
      [sv] "=&s"(sv_), [t] "=&s"(t_), [cnt] "=&s"(cnt_) \
      : [o] "v"(off), [rs] "s"(rsd), [tag] "s"(tagv), [act] "s"(actv) : GCLOB); } while (0)

#define GL2(R, K) "buffer_load_dwordx4 v[" #R "+4*" #K ":" #R "+4*" #K "+3], %[o2], %[rs], 0 offen sc1\n\t"
#define G3L(R) GL(R, 0, 128) GL(R, 1, 128) GL2(R, 2)
#define G3C(R) GC(R, 0, a0, b0) GC(R, 1, a1, b1) "v_cmp_eq_u32 %[t], %[tag], v[" #R "+9]\n\ts_and_b64 vcc, vcc, %[t]\n\tv_mov_b32 %[a2], v[" #R "+8]\n\t"
#define GATHER3(SL, rsd, off, off2, tagv, actv, D) do { u64 sv_, t_; unsigned cnt_; \
    asm volatile(GBODY(G3L, G3C, 6, 3, SL) : [a0] "+v"(D[0]), [b0] "+v"(D[1]), [a1] "+v"(D[2]), [b1] "+v"(D[3]), [a2] "+v"(D[4]), [sv] "=&s"(sv_), [t] "=&s"(t_), [cnt] "=&s"(cnt_) \
      : [o] "v"(off), [o2] "v"(off2), [rs] "s"(rsd), [tag] "s"(tagv), [act] "s"(actv) : GCLOB); } while (0)
#define GLS(R, K, OFFS, SOFF) "buffer_load_dwordx4 v[" #R "+4*" #K ":" #R "+4*" #K "+3], %[o], %[rs], " SOFF " offen offset:" #OFFS " sc1\n\t"
#define G4CL(R) GLS(R, 0, 0, "0") GLS(R, 1, 2048, "0") GLS(R, 2, 0, "%[so]") GLS(R, 3, 2048, "%[so]")
#define G4DL(R) GLS(R, 0, 0, "0") GLS(R, 1, 1792, "0") GLS(R, 2, 0, "%[so]") GLS(R, 3, 1792, "%[so]")
#define GATHER4S(LOADM, SL, rsd, off, sov, tagv, actv, D) do { u64 sv_, t_; unsigned cnt_; \
    asm volatile(GBODY(LOADM, G4C, 8, 4, SL) : [a0] "+v"(D[0]), [b0] "+v"(D[1]), [a1] "+v"(D[2]), [b1] "+v"(D[3]), [a2] "+v"(D[4]), [b2] "+v"(D[5]), [a3] "+v"(D[6]), [b3] "+v"(D[7]), \
      [sv] "=&s"(sv_), [t] "=&s"(t_), [cnt] "=&s"(cnt_) \
      : [o] "v"(off), [rs] "s"(rsd), [so] "s"(sov), [tag] "s"(tagv), [act] "s"(actv) : GCLOB); } while (0)
#define GATHER4SX(a, b, c, d, e, f, g, h) GATHER4S(a, b, c, d, e, f, g, h)
#define GATHER4X(a, b, c, d, e, f, g) GATHER4(a, b, c, d, e, f, g)
__device__ __forceinline__ v4u ln_row(v4u in) {
  float v[8];
#pragma unroll
  for (int k = 0; k < 4; ++k) { v[2 * k] = lo16(in[k]); v[2 * k + 1] = hi16(in[k]); }
  const float m = xrow_sum(row16_sum(((v[0] + v[1]) + (v[2] + v[3])) + ((v[4] + v[5]) + (v[6] + v[7])))) * (1.f / 512.f);
  float q = 0.f;
#pragma unroll
  for (int j = 0; j < 8; ++j) { const float d = v[j] - m; q = fmaf(d, d, q); }
  const float var = xrow_sum(row16_sum(q)) * (1.f / 512.f);
  const float mr = rbf(m);
  const float den = rbf(__builtin_sqrtf(rbf(rbf(var) + 1e-5f)));
  const float inv = 1.f / den;
  v4u o;
#pragma unroll
  for (int k = 0; k < 4; ++k) {
    const unsigned d = pk(v[2 * k] - mr, v[2 * k + 1] - mr);
    o[k] = pk(lo16(d) * inv, hi16(d) * inv);
  }
  return o;
}

__device__ __forceinline__ void tr8(const v4u* in, v4u (&out)[8]) {
#pragma unroll
  for (int m = 0; m < 4; ++m)
#pragma unroll
    for (int jp = 0; jp < 4; ++jp) {
      out[2 * m][jp] = __builtin_amdgcn_perm(in[jp + 4][m], in[jp][m], 0x05040100u);
      out[2 * m + 1][jp] = __builtin_amdgcn_perm(in[jp + 4][m], in[jp][m], 0x07060302u);
    }
}

template <int SLEEP = 0> __device__ __forceinline__ void lds_wait4(LAS unsigned char* p) {
  for (unsigned spins = 0; spins < (SPIN_CAP << 4); ++spins) {
    const v4u f = *(volatile LAS v4u*)p;
    if (__builtin_amdgcn_readfirstlane(f.x & f.y & f.z & f.w) != 0) break;
    if (SLEEP) __builtin_amdgcn_s_sleep(SLEEP);
  }
  CB();
}

#define SETFLAG(F) do { CB(); *(volatile LAS unsigned*)(L + (F) + w * 4) = 1u; } while (0)

__global__ void __launch_bounds__(512, 2)
fused_kernel(const u16* __restrict__ x, const u16* __restrict__ kc, const u16* __restrict__ vc, const u16* __restrict__ wq,
             const u16* __restrict__ wo, const u16* __restrict__ w1, const u16* __restrict__ b1, const u16* __restrict__ w2,
             const u16* __restrict__ b2, u16* __restrict__ out, unsigned char* __restrict__ ws) {
  __shared__ __attribute__((aligned(16))) unsigned char smem[LDS_BYTES + 16];
  u64 tstart; asm volatile("s_memrealtime %0" : "=s"(tstart));
  LAS unsigned char* L = (LAS unsigned char*)smem;
  STAMP(0);
  const int tid = threadIdx.x, lane = tid & 63, wave = __builtin_amdgcn_readfirstlane(tid >> 6);
  const int wg = blockIdx.x, kvh = wg & 7, r = wg >> 3, b = r >> 3, s = r & 7;
  const int l16 = lane & 15, c = lane >> 4;
  const int ks = wg >> 2, ns = wg & 3;
  if (tid < 44) *(LAS unsigned*)(L + L_FLAG + tid * 4) = 0u;
  unsigned pf = 0;
  if (wave == 3) {
    u64 pc; unsigned len;
    asm volatile("s_getpc_b64 %0\n.Lpc_here:\n\ts_mov_b32 %1, .Lk_end-.Lpc_here" : "=s"(pc), "=s"(len));
    pf = ld32<0>(mkrs((const void*)pc, len), r * 512 + lane * 8, 0);
  }
  asm volatile("" :: "s"(x), "s"(kc), "s"(vc), "s"(wq), "s"(wo), "s"(w1), "s"(b1), "s"(w2), "s"(b2), "s"(out), "s"(ws));
  BAR();
  const rsrc_t rws = mkrs(ws, WS_BYTES);
  const unsigned ep = *(const __attribute__((address_space(4))) unsigned*)(ws + EP_OFF);
  unsigned tag;
#define MAKE_TAG() asm volatile("s_add_u32 %0, %1, 1" : "=s"(tag) : "s"(ep) : "scc")
  const int w = wave & 3;
  const size_t kvbase = ((size_t)(b * 8 + kvh) * 8192 + 6144) * 128;
  int tso[4];
#pragma unroll
  for (int t = 0; t < 4; ++t) tso[t] = (32 * t + ((4 * s + w + ROT * t) & 31)) * 4096;
  const int dup = l16 & 3;
  const int u1 = kvh * 14 + r;
  const int kq = r & 3, kq1 = u1 & 3;

  v4u xr, wv[4], uv[4], K[16];
  const rsrc_t rk = mkrs(kc + kvbase, 2048 * 256);
  const int koff = (l16 >> 2) * 256 + c * 64 + dup * 16;
  if (wave >= 4) {
    __builtin_amdgcn_s_setprio(VPRIO);
    const rsrc_t rwq = mkrs(wq + kvh * 512 + (r >> 2) * 64, (EXP & 2) ? 0 : 512 * 8192);
    const rsrc_t rw1 = mkrs(w1 + (u1 >> 2) * 64, (r < 14 && !(EXP & 1)) ? 512 * 3584 : 0);
    xr = ld128<0>(mkrs(x, 4096), w * 1024 + lane * 16, 0);
    SB();
#pragma unroll
    for (int n = 0; n < 4; ++n) wv[n] = ld128<AUX_NT>(rwq, (lane >> 3) * 8192 + (lane & 7) * 16, (kq * 128 + w * 32 + n * 8) * 8192);
    SB();
#pragma unroll
    for (int n = 0; n < 4; ++n) uv[n] = ld128<AUX_NT>(rw1, (lane >> 3) * 3584 + (lane & 7) * 16, (kq1 * 128 + w * 32 + n * 8) * 3584);
    SB();
  } else {
    __builtin_amdgcn_s_sleep(KDELAY);
#pragma unroll
    for (int n = 0; n < WK; ++n) K[n] = ld128<AUX_NT>(rk, koff + (n & 3) * 1024, tso[n >> 2]);
    SB();
  }
  int wave2 = wave;
  asm volatile("" : "+s"(wave2));

  if (wave2 < 4) {
    const int qoff = QP_OFF + ((kvh * 4 + b) * 4 + w) * 4096 + lane * 16;
    v4u Q[4];
#pragma unroll
    for (int n = WK; n < 16; ++n) {
      asm volatile("s_waitcnt vmcnt(%0)" :: "n"(WK - 1) : "memory");
      K[n] = ld128<AUX_NT>(rk, koff + (n & 3) * 1024, tso[n >> 2]);
      SB();
      if (n == QAT) {
#pragma unroll
        for (int k = 0; k < 4; ++k) Q[k] = ld128<AUX_SC1>(rws, qoff + k * 1024, 0);
        SB();
      }
    }
    MAKE_TAG();
    SETFLAG(F_K);
    __builtin_amdgcn_s_setprio(KPRIO);
    asm volatile("" :: "v"(pf));
    STAMP(1);
    {
      float qd[8];
      unsigned bad = 0;
#pragma unroll
      for (int k = 0; k < 4; ++k) { qd[2 * k] = __uint_as_float(Q[k].x); qd[2 * k + 1] = __uint_as_float(Q[k].z); bad |= (Q[k].y ^ tag) | (Q[k].w ^ tag); }
      const u64 miss = __builtin_amdgcn_ballot_w64(bad != 0);
      if (miss) GATHER4X(G4AL, QSL, rws, qoff, tag, miss, qd);
      *(LAS unsigned*)(L + L_Q + w * 256 + lane * 4) = pk((qd[0] + qd[2]) + (qd[4] + qd[6]), (qd[1] + qd[3]) + (qd[5] + qd[7]));
      SETFLAG(F_Q);
    }
    v4u Z[4];
    unsigned bias = 0;
    const int zoff = wg < 224 ? ZP_OFF + ks * 4096 + lane * 16 : OOB;
    if (wave2 == 3) {
#pragma unroll
      for (int k = 0; k < 4; ++k) Z[k] = ld128<AUX_SC1>(rws, zoff + k * 1024, 0);
      bias = ld32<0>(mkrs(b1, 3584), (ks * 32 + l16 * 2) * 2, 0);
      SB();
    }
    STAMP(2);
    lds_wait4(L + F_Q);
    STAMP(13);
    const v4u qf = *(LAS v4u*)(L + L_Q + dup * 256 + c * 64 + (l16 >> 2) * 16);
    float sc[4];
#pragma unroll
    for (int mm = 0; mm < 4; ++mm) {
      float t[4];
#pragma unroll
      for (int be = 0; be < 4; ++be) {
        const f32x4 d = mfma(K[4 * mm + be], qf, f32x4{0.f, 0.f, 0.f, 0.f});
        t[be] = ((d[be] + dpp<0x12C>(d[(be + 1) & 3])) + dpp<0x128>(d[(be + 2) & 3])) + dpp<0x124>(d[(be + 3) & 3]);
      }
      const float t01 = (l16 & 4) ? t[1] : t[0], t23 = (l16 & 4) ? t[3] : t[2];
      sc[mm] = (l16 & 8) ? t23 : t01;
    }
    {
      const unsigned d0 = pk(sc[0], sc[1]), d1 = pk(sc[2], sc[3]);
      const unsigned e0 = pk(lo16(d0) * 0.08838834764831845f, hi16(d0) * 0.08838834764831845f);
      const unsigned e1 = pk(lo16(d1) * 0.08838834764831845f, hi16(d1) * 0.08838834764831845f);
      sc[0] = lo16(e0); sc[1] = hi16(e0); sc[2] = lo16(e1); sc[3] = hi16(e1);
    }
    float m = fmaxf(fmaxf(sc[0], sc[1]), fmaxf(sc[2], sc[3]));
    m = fmaxf(m, dpp<0x124>(m)); m = fmaxf(m, dpp<0x128>(m));
    m = xrow_max(m);
    const float p0 = ex2((sc[0] - m) * LOG2E), p1 = ex2((sc[1] - m) * LOG2E), p2 = ex2((sc[2] - m) * LOG2E), p3 = ex2((sc[3] - m) * LOG2E);
    *(LAS unsigned*)(L + L_P + w * 512 + dup * 64 + c * 16 + (l16 >> 2) * 4) = pk(p0, p1);
    *(LAS unsigned*)(L + L_P + w * 512 + 256 + dup * 64 + c * 16 + (l16 >> 2) * 4) = pk(p2, p3);
    float l = (p0 + p1) + (p2 + p3);
    l += dpp<0x124>(l); l += dpp<0x128>(l);
    l = xrow_sum(l);
    if (lane < 4) *(LAS f32x2*)(L + L_ST + lane * 32 + w * 8) = f32x2{m, l};
    SETFLAG(F_P);
    STAMP(3);
    if (wave2 == 3) {
      float zd[8];
      unsigned bad = 0;
#pragma unroll
      for (int k = 0; k < 4; ++k) { zd[2 * k] = __uint_as_float(Z[k].x); zd[2 * k + 1] = __uint_as_float(Z[k].z); bad |= (Z[k].y ^ tag) | (Z[k].w ^ tag); }
      const u64 miss = __builtin_amdgcn_ballot_w64(bad != 0);
      if (wg < 224 && miss) GATHER4(G4AL, 8, rws, zoff, tag, miss, zd);
      const unsigned zp = pk((zd[0] + zd[2]) + (zd[4] + zd[6]), (zd[1] + zd[3]) + (zd[5] + zd[7]));
      const unsigned zb = pk(lo16(zp) + lo16(bias), hi16(zp) + hi16(bias));
      float z[2] = {lo16(zb), hi16(zb)};
#pragma unroll
      for (int i = 0; i < 2; ++i) {
        const float zz = z[i];
        const float u = 0.7978845608028654f * (zz + 0.044715f * zz * zz * zz);
        const float th = 1.f - 2.f * rcp(ex2(u * (2.f * LOG2E)) + 1.f);
        z[i] = 0.5f * zz * (1.f + th);
      }
      *(LAS unsigned*)(L + L_H + lane * 4) = pk(z[0], z[1]);
      CB();
      if (lane < 4) *(volatile LAS unsigned*)(L + F_H + lane * 4) = 1u;
    }
    STAMP(4);
    const int oidx = (wg >> 6) * 512 + (wg & 7) * 64 + ((wg >> 5) & 1) * 32 + ((wg >> 3) & 3) * 4 + ((lane >> 1) & 1) * 16 + (lane & 1) * 2;
    const unsigned xw = ld32<0>(mkrs(x, 4096), oidx * 2, 0);
    const unsigned bw = ld32<0>(mkrs(b2, 1024), (oidx & 511) * 2, 0);
    const int sp = lane & 7, k4 = lane >> 3;
    const int osw = L_OS + w * OS_STRIDE + k4 * 8;
    const int p1g = P1_OFF + (ks * 32 + ((w * 8 + sp + SKEW * ks) & 31)) * 256 + k4 * 16;
    const int l1g = L1_OFF + (((ks >> 1) * 4 + w) * 8 + sp) * 128;
    asm volatile("" :: "v"(osw), "v"(l1g), "v"(p1g), "v"(xw), "v"(bw));
    lds_wait4<2>(L + F_O);
    __builtin_amdgcn_s_sleep(PSL);
    STAMP(12);
    unsigned d[5] = {0u, 0u, 0u, 0u, 0u};
    GATHER3(4, rws, p1g, l1g, tag, ~0ull, d);
    STAMP(6);
    {
      const float lse = __uint_as_float(d[4]);
      const float e = ex2(lse - oct_max(lse));
      const float inv = rcp(oct_sum(e));
      float a[8];
#pragma unroll
      for (int k = 0; k < 4; ++k) { a[2 * k] = oct_sum(e * lo16(d[k])) * inv; a[2 * k + 1] = oct_sum(e * hi16(d[k])) * inv; }
      if (sp == 0) {
        *(LAS v2u*)(L + osw) = v2u{pk(a[0], a[1]), pk(a[2], a[3])};
        *(LAS v2u*)(L + osw + 64) = v2u{pk(a[4], a[5]), pk(a[6], a[7])};
      }
      SETFLAG(F_OS);
    }
    const int foff = ((wg >> 7) * 64 + (wg & 63)) * 8192 + ((wg >> 6) & 1) * 64 + (lane >> 2) * 128 + (lane & 3) * 16;
    if (wave2 == 1) {
      float dg[8] = {0.f, 0.f, 0.f, 0.f, 0.f, 0.f, 0.f, 0.f};
      GATHER4SX(G4DL, GPS, rws, G2_OFF + foff, 3584, tag, (1ull << 56) - 1ull, dg);
      float g0 = (dg[0] + dg[2]) + (dg[4] + dg[6]);
      float g1 = (dg[1] + dg[3]) + (dg[5] + dg[7]);
      g0 += dpp<0x124>(g0); g1 += dpp<0x124>(g1);
      g0 += dpp<0x128>(g0); g1 += dpp<0x128>(g1);
      g0 = xrow_sum(g0); g1 = xrow_sum(g1);
      const unsigned gp = pk(g0, g1);
      const unsigned gb = pk(lo16(gp) + lo16(bw), hi16(gp) + hi16(bw));
      const float z0 = lo16(gb), z1 = hi16(gb);
      const unsigned sg = pk(z0 * rcp(1.f + ex2(-z0 * LOG2E)), z1 * rcp(1.f + ex2(-z1 * LOG2E)));
      if (lane < 4) *(volatile LAS v2u*)(L + F_G + lane * 8) = v2u{sg, 1u};
      STAMP(8);
    } else if (wave2 == 0) {
      __builtin_amdgcn_s_sleep(FSL);
      float ds[8] = {0.f, 0.f, 0.f, 0.f, 0.f, 0.f, 0.f, 0.f};
      GATHER4SX(G4CL, FPS, rws, P2_OFF + foff, 4096, tag, ~0ull, ds);
      STAMP(9);
      st128(rws, (wg == 0 && lane < 4) ? EP_OFF + lane * 16 : OOB, v4u{tag, tag, tag, tag});
      v2u sgt = *(volatile LAS v2u*)(L + F_G + (lane & 3) * 8);
      float s0 = (ds[0] + ds[2]) + (ds[4] + ds[6]);
      float s1 = (ds[1] + ds[3]) + (ds[5] + ds[7]);
      s0 += dpp<0x124>(s0); s1 += dpp<0x124>(s1);
      s0 += dpp<0x128>(s0); s1 += dpp<0x128>(s1);
      s0 = xrow_sum(s0); s1 = xrow_sum(s1);
      const unsigned sq = pk(s0, s1);
      for (unsigned spins = 0; !__all(sgt.y != 0u) && spins < (SPIN_CAP << 4); ++spins) sgt = *(volatile LAS v2u*)(L + F_G + (lane & 3) * 8);
      const unsigned pr = pk(lo16(sq) * lo16(sgt.x), hi16(sq) * hi16(sgt.x));
      const unsigned res = pk(lo16(xw) + lo16(pr), hi16(xw) + hi16(pr));
      if (lane < 4) *(GAS unsigned*)(out + oidx) = res;
      STAMP(10);
      {
        const u64 tx = PADN >= 0 ? *(volatile LAS u64*)(L + LDS_BYTES) : tstart;
        const u64 target = tx + 4000;
        for (;;) { u64 t_; asm volatile("s_memrealtime %0\n\ts_waitcnt lgkmcnt(0)" : "=s"(t_) :: "memory"); if (t_ >= target) break; __builtin_amdgcn_s_sleep(1); }
      }
    }
  } else {
    const v4u xn = ln_row(xr);
    *(LAS v4u*)(L + L_XN + w * XS_STRIDE + lane * 16) = xn;
    MAKE_TAG();
    STAMP(1);
    const int sw = (w * 32 + ((lane >> 5) & 1) * 8 + ((lane >> 3) & 3)) * WT_RS + (lane & 7) * 16;
#pragma unroll
    for (int n = 0; n < 4; ++n) *(LAS v4u*)(L + L_WQ + sw + ((n >> 1) * 16 + (n & 1) * 4) * WT_RS) = wv[n];
    SETFLAG(F_A);
    const int ta = ((c >> 1) * 16 + (c & 1) * 4 + (l16 >> 2)) * WT_RS + w * 32 + (l16 & 3) * 8;
    const int xb = (l16 >> 2) * XS_STRIDE + c * 16;
    lds_wait4(L + F_A);
    {
      f32x4 acc = {0.f, 0.f, 0.f, 0.f};
#pragma unroll
      for (int kk = 0; kk < 4; ++kk) {
        const v2u a0 = trread(L + L_WQ + ta + kk * 32 * WT_RS), a1 = trread(L + L_WQ + ta + (kk * 32 + 8) * WT_RS);
        acc = mfma(*(LAS v4u*)(L + L_XN + xb + kq * 256 + kk * 64), v4u{a0.x, a0.y, a1.x, a1.y}, acc);
      }
      st64(rws, QP_OFF + ((((kvh * 4 + c) * 4 + (r >> 3)) * 4 + kq) * 128 + ((r >> 2) & 1) * 64 + w * 16 + l16) * 8, v2u{__float_as_uint(acc[0]), tag});
    }
    SB();
    STAMP(2);
    const v4u yn = ln_row(xn);
    *(LAS v4u*)(L + L_YN + w * XS_STRIDE + lane * 16) = yn;
#pragma unroll
    for (int n = 0; n < 4; ++n) *(LAS v4u*)(L + L_W1 + sw + ((n >> 1) * 16 + (n & 1) * 4) * WT_RS) = uv[n];
    SETFLAG(F_B);
    lds_wait4(L + F_B);
    {
      f32x4 acc = {0.f, 0.f, 0.f, 0.f};
#pragma unroll
      for (int kk = 0; kk < 4; ++kk) {
        const v2u a0 = trread(L + L_W1 + ta + kk * 32 * WT_RS), a1 = trread(L + L_W1 + ta + (kk * 32 + 8) * WT_RS);
        acc = mfma(*(LAS v4u*)(L + L_YN + xb + kq1 * 256 + kk * 64), v4u{a0.x, a0.y, a1.x, a1.y}, acc);
      }
      const int off = ZP_OFF + (((((u1 >> 2) * 2 + (w >> 1)) * 4 + kq1) * 4 + c) * 32 + (w & 1) * 16 + l16) * 8;
      st64(rws, r < 14 ? off : OOB, v2u{__float_as_uint(acc[0]), tag});
    }
    __builtin_amdgcn_s_setprio(0);
    STAMP(3);
    for (unsigned spins = 0; spins < (SPIN_CAP << 4); ++spins) {
      if (*(volatile LAS unsigned*)(L + F_K + w * 4) != 0u) break;
      __builtin_amdgcn_s_sleep(1);
    }
    const rsrc_t rv = mkrs(vc + kvbase, 2048 * 256);
    const int voff = lane * 16;
    const rsrc_t rwo = mkrs(wo + (size_t)ks * 64 * 512 + ns * 128, 64 * 1024);
    const rsrc_t rw2 = mkrs(w2 + (size_t)ks * 32 * 512 + ns * 128, wg < 224 ? 32 * 1024 : 0);
    v4u V[16], WO[6];
#pragma unroll
    for (int n = 0; n < 16; ++n) {
      if (n >= WV) asm volatile("s_waitcnt vmcnt(%0)" :: "n"(WV - 1) : "memory");
      V[n] = ld128<AUX_NT>(rv, voff + (n & 3) * 1024, tso[n >> 2]);
      SB();
      if (n < WV - 1 && VRAMP) { __builtin_amdgcn_s_sleep(VRAMP); SB(); }
    }
    STAMP(4);
#define WO_LOAD(n) do { asm volatile("s_waitcnt vmcnt(%0)" :: "n"(WV - 1) : "memory"); \
      WO[n] = (n) < 4 ? ld128<AUX_NT>(rwo, (16 * w + c) * 1024 + l16 * 16, (n) * 4096) : ld128<AUX_NT>(rw2, (8 * w + c) * 1024 + l16 * 16, ((n) - 4) * 4096); SB(); } while (0)
    WO_LOAD(0); WO_LOAD(1);
    lds_wait4<1>(L + F_P);
    v4u P[2];
    P[0] = *(LAS v4u*)(L + L_P + w * 512 + (l16 >> 2) * 64 + c * 16);
    P[1] = *(LAS v4u*)(L + L_P + w * 512 + 256 + (l16 >> 2) * 64 + c * 16);
    float wgt[4];
    unsigned lse2;
    {
      const f32x4 s0 = *(LAS f32x4*)(L + L_ST + w * 32), s1 = *(LAS f32x4*)(L + L_ST + w * 32 + 16);
      const float M = fmaxf(fmaxf(s0[0], s0[2]), fmaxf(s1[0], s1[2]));
      wgt[0] = ex2((s0[0] - M) * LOG2E);
      wgt[1] = ex2((s0[2] - M) * LOG2E);
      wgt[2] = ex2((s1[0] - M) * LOG2E);
      wgt[3] = ex2((s1[2] - M) * LOG2E);
      const float Ls = wgt[0] * s0[1] + wgt[1] * s0[3] + wgt[2] * s1[1] + wgt[3] * s1[3];
      const float inv = rcp(Ls);
#pragma unroll
      for (int k = 0; k < 4; ++k) wgt[k] *= inv;
      lse2 = __float_as_uint(M * LOG2E + __builtin_amdgcn_logf(Ls));
    }
    const int ksp = (kvh * 4 + w) * 2 + (c & 1);
    const int p1off = lane < 32 ? P1_OFF + (ksp * 32 + ((r + SKEW * ksp) & 31)) * 256 + l16 * 16
                    : lane < 40 ? L1_OFF + (((kvh * 4 + w) * 4 + b) * 8 + s) * 128 + l16 * 16 : OOB;
    const int ppw = L_PART + w * 2048 + c * 512 + l16 * 32, ppr = L_PART + w * 512 + (lane & 31) * 16;
    f32x4 o[8];
    {
      v4u vt[8];
      tr8(&V[0], vt);
#pragma unroll
      for (int e = 0; e < 8; ++e) o[e] = mfma(P[0], vt[e], f32x4{0.f, 0.f, 0.f, 0.f});
    }
    asm volatile("" :: "v"(p1off), "v"(ppw), "v"(ppr), "v"(wgt[0]), "v"(wgt[1]), "v"(wgt[2]), "v"(wgt[3]), "v"(lse2));
    SB();
    WO_LOAD(2); WO_LOAD(3); WO_LOAD(4); WO_LOAD(5);
    {
      v4u vt[8];
      tr8(&V[8], vt);
#pragma unroll
      for (int e = 0; e < 8; ++e) o[e] = mfma(P[1], vt[e], o[e]);
    }
    ((LAS f32x4*)(L + ppw))[0] = f32x4{o[0][0], o[1][0], o[2][0], o[3][0]};
    ((LAS f32x4*)(L + ppw))[1] = f32x4{o[4][0], o[5][0], o[6][0], o[7][0]};
    SETFLAG(F_O);
    STAMP(11);
    lds_wait4(L + F_O);
    {
      const LAS f32x4* rp = (const LAS f32x4*)(L + ppr);
      const f32x4 a = (wgt[0] * rp[0] + wgt[1] * rp[128]) + (wgt[2] * rp[256] + wgt[3] * rp[384]);
      st128(rws, p1off, v4u{lane < 32 ? pk(a[0], a[1]) : lse2, tag, pk(a[2], a[3]), tag});
    }
    STAMP(5);
#pragma unroll
    for (int n = 0; n < 4; ++n) *(LAS v4u*)(L + L_WO + swap23(16 * w + 4 * n + c) * WO_RS + l16 * 16) = WO[n];
#pragma unroll
    for (int n = 0; n < 2; ++n) *(LAS v4u*)(L + L_WO + (64 + swap23(8 * w + 4 * n + c)) * WO_RS + l16 * 16) = WO[4 + n];
    SETFLAG(F_WO);
    STAMP(14);
    const int fpair = (ns * 2 + (w >> 1)) + 8 * ((l16 >> 3) * 8 + (w & 1) * 4 + c);
    const int sk = ks + SKEW * (fpair >> 3);
    const int p2off = P2_OFF + (fpair * 64 + (sk & 63)) * 128 + (l16 & 7) * 16;
    const int g2off = G2_OFF + (fpair * 64 + (sk >= 56 ? sk - 56 : sk)) * 128 + (l16 & 7) * 16;
    const int osr = L_OS + (l16 >> 2) * OS_STRIDE + c * 16;
    lds_wait4(L + F_WO);
    v4u af[2][2];
    {
      LAS unsigned char* tw = L + L_WO + ((c >> 1) * 16 + (c & 1) * 4 + (l16 >> 2)) * WO_RS + w * 64 + (l16 & 3) * 8;
#pragma unroll
      for (int ct = 0; ct < 2; ++ct)
#pragma unroll
        for (int kk = 0; kk < 2; ++kk) {
          const v2u a0 = trread(tw + ct * 32 + kk * 32 * WO_RS), a1 = trread(tw + ct * 32 + kk * 32 * WO_RS + 8 * WO_RS);
          af[ct][kk] = v4u{a0.x, a0.y, a1.x, a1.y};
        }
      lds_wait4<1>(L + F_H);
      const v4u hb = *(LAS v4u*)(L + L_H + (l16 >> 2) * 64 + c * 16);
      f32x4 g[2];
#pragma unroll
      for (int ct = 0; ct < 2; ++ct) {
        const v2u a0 = trread(tw + ct * 32 + 64 * WO_RS), a1 = trread(tw + ct * 32 + 72 * WO_RS);
        g[ct] = mfma(v4u{a0.x, a0.y, a1.x, a1.y}, hb, f32x4{0.f, 0.f, 0.f, 0.f});
      }
      const f32x4 acc = (dup & 2) ? g[1] : g[0];
      const float v0 = (dup & 1) ? acc[2] : acc[0], v1 = (dup & 1) ? acc[3] : acc[1];
      st128(rws, wg < 224 ? g2off : OOB, v4u{__float_as_uint(v0), tag, __float_as_uint(v1), tag});
    }
    asm volatile("" :: "v"(p2off), "v"(osr), "v"(af[0][0]), "v"(af[0][1]), "v"(af[1][0]), "v"(af[1][1]));
    STAMP(6);
    lds_wait4(L + F_OS);
    {
      const v4u b0 = *(LAS v4u*)(L + osr), b1f = *(LAS v4u*)(L + osr + 64);
      f32x4 acc0 = mfma(af[0][0], b0, f32x4{0.f, 0.f, 0.f, 0.f});
      f32x4 acc1 = mfma(af[1][0], b0, f32x4{0.f, 0.f, 0.f, 0.f});
      acc0 = mfma(af[0][1], b1f, acc0);
      acc1 = mfma(af[1][1], b1f, acc1);
      const f32x4 acc = (dup & 2) ? acc1 : acc0;
      const float v0 = (dup & 1) ? acc[2] : acc[0], v1 = (dup & 1) ? acc[3] : acc[1];
      st128(rws, p2off, v4u{__float_as_uint(v0), tag, __float_as_uint(v1), tag});
    }
    STAMP(7);
  }
  asm volatile(".Lk_end:");
}

extern "C" void kernel_launch(void* const* d_in, const int* in_sizes, int n_in,
                              void* d_out, int out_size, void* d_ws, size_t ws_size,
                              hipStream_t stream) {
  fused_kernel<<<256, 512, 0, stream>>>((const u16*)d_in[0], (const u16*)d_in[1], (const u16*)d_in[2], (const u16*)d_in[3],
                                        (const u16*)d_in[4], (const u16*)d_in[6], (const u16*)d_in[7], (const u16*)d_in[8],
                                        (const u16*)d_in[9], (u16*)d_out, (unsigned char*)d_ws);
}
```

```cpp
#include <hip/hip_runtime.h>
#include <stdint.h>

#define GAS __attribute__((address_space(1)))
#define LAS __attribute__((address_space(3)))
typedef unsigned short u16;
typedef unsigned long long u64;
typedef unsigned v4u __attribute__((ext_vector_type(4)));
typedef unsigned v2u __attribute__((ext_vector_type(2)));
typedef short v4s __attribute__((ext_vector_type(4)));
typedef float f32x4 __attribute__((ext_vector_type(4)));
typedef float f32x2 __attribute__((ext_vector_type(2)));
typedef __bf16 v8bf __attribute__((ext_vector_type(8)));
typedef __bf16 v2bf __attribute__((ext_vector_type(2)));
typedef __amdgpu_buffer_rsrc_t rsrc_t;

constexpr int AUX_NT = 0, AUX_SC1 = 16;
constexpr unsigned SPIN_CAP = 1u << 20;
constexpr int OOB = 0x7ffffff0;
constexpr float LOG2E = 1.4426950408889634f;

#define EXP 0
#define QSL 8
#define QAT 15
#define VWAITK 1
#define FSL 30
#define KDELAY 6
#define GSL 20
#define VPRIO 3
#define LSL 16
#define PSL 12
#define VRAMP 4
#define ROT 9
#define SKEW 2
#define KPRIO 2
#define GPS 4
#define FPS 3
#define GMODE 1
#define WOE 0
#define WOAT 1
#define PFROT 0
#define PFAUX 0
#define QW 6
#define VRN 9
#define WK 6
#define WV 10
constexpr int EP_OFF = 0;
constexpr int QP_OFF = 4096;
constexpr int ZP_OFF = QP_OFF + 524288;
constexpr int L1_OFF = ZP_OFF + 458752;
constexpr int P1_OFF = L1_OFF + 131072;
constexpr int P2_OFF = P1_OFF + 524288;
constexpr int G2_OFF = P2_OFF + 1048576;
constexpr int WS_BYTES = G2_OFF + 1048576;
constexpr int ST_OFF = 8 << 20;

constexpr int XS_STRIDE = 1024 + 64;
constexpr int WT_RS = 160;
constexpr int L_WQ = 0;
constexpr int L_W1 = L_WQ + 128 * WT_RS;
constexpr int L_XN = L_W1 + 64 * WT_RS;
constexpr int L_YN = L_XN + 4 * XS_STRIDE;
constexpr int L_Q = L_YN + 4 * XS_STRIDE;
constexpr int L_ST = L_Q + 1024;
constexpr int L_FLAG = L_ST + 128;
constexpr int F_A = L_FLAG, F_B = L_FLAG + 16, F_Q = L_FLAG + 32, F_K = L_FLAG + 48, F_P = L_FLAG + 64, F_O = L_FLAG + 80, F_WO = L_FLAG + 96, F_OS = L_FLAG + 112, F_G = L_FLAG + 128, F_H = L_FLAG + 160;
constexpr int L_P = L_FLAG + 176;
constexpr int L_PART = L_P + 2048;
constexpr int WO_RS = 288;
constexpr int L_WO = L_PART + 8192;
constexpr int OS_STRIDE = 128 + 16;
constexpr int L_OS = L_WO + 96 * WO_RS;
constexpr int L_H = L_OS + 4 * OS_STRIDE;
constexpr int LDS_BYTES = L_H + 256;

__device__ __forceinline__ unsigned pk(float lo, float hi) { f32x2 v = {lo, hi}; return __builtin_bit_cast(unsigned, __builtin_convertvector(v, v2bf)); }
__device__ __forceinline__ float lo16(unsigned u) { return __builtin_bit_cast(float, u << 16); }
__device__ __forceinline__ float hi16(unsigned u) { return __builtin_bit_cast(float, u & 0xffff0000u); }
__device__ __forceinline__ float rbf(float x) { return lo16(pk(x, x)); }
__device__ __forceinline__ float rcp(float x) { return __builtin_amdgcn_rcpf(x); }
__device__ __forceinline__ float ex2(float x) { return __builtin_amdgcn_exp2f(x); }
template <int CTRL> __device__ __forceinline__ float dpp(float x) {
  return __builtin_bit_cast(float, __builtin_amdgcn_mov_dpp(__builtin_bit_cast(int, x), CTRL, 0xf, 0xf, true));
}
__device__ __forceinline__ float row16_sum(float x) { x += dpp<0x128>(x); x += dpp<0x124>(x); x += dpp<0x122>(x); x += dpp<0x121>(x); return x; }
__device__ __forceinline__ float oct_sum(float x) { x += dpp<0xB1>(x); x += dpp<0x4E>(x); x += dpp<0x141>(x); return x; }
__device__ __forceinline__ float oct_max(float x) { x = fmaxf(x, dpp<0xB1>(x)); x = fmaxf(x, dpp<0x4E>(x)); x = fmaxf(x, dpp<0x141>(x)); return x; }
__device__ __forceinline__ float xrow_max(float x) {
  auto s = __builtin_amdgcn_permlane16_swap(__float_as_uint(x), __float_as_uint(x), false, false);
  x = fmaxf(__uint_as_float(s[0]), __uint_as_float(s[1]));
  auto t = __builtin_amdgcn_permlane32_swap(__float_as_uint(x), __float_as_uint(x), false, false);
  return fmaxf(__uint_as_float(t[0]), __uint_as_float(t[1]));
}
__device__ __forceinline__ float xrow_sum(float x) {
  auto s = __builtin_amdgcn_permlane16_swap(__float_as_uint(x), __float_as_uint(x), false, false);
  x = __uint_as_float(s[0]) + __uint_as_float(s[1]);
  auto t = __builtin_amdgcn_permlane32_swap(__float_as_uint(x), __float_as_uint(x), false, false);
  return __uint_as_float(t[0]) + __uint_as_float(t[1]);
}
__device__ __forceinline__ f32x4 mfma(v4u a, v4u b, f32x4 c) {
  return __builtin_amdgcn_mfma_f32_16x16x32_bf16(__builtin_bit_cast(v8bf, a), __builtin_bit_cast(v8bf, b), c, 0, 0, 0);
}
__device__ __forceinline__ rsrc_t mkrs(const void* p, int bytes) { return __builtin_amdgcn_make_buffer_rsrc((void*)p, 0, bytes, 0x00020000); }
template <int AUX> __device__ __forceinline__ v4u ld128(rsrc_t r, int voff, int soff) { return __builtin_amdgcn_raw_buffer_load_b128(r, voff, soff, AUX); }
template <int AUX> __device__ __forceinline__ v2u ld64(rsrc_t r, int voff, int soff) { return __builtin_amdgcn_raw_buffer_load_b64(r, voff, soff, AUX); }
template <int AUX> __device__ __forceinline__ unsigned ld32(rsrc_t r, int voff, int soff) { return __builtin_amdgcn_raw_buffer_load_b32(r, voff, soff, AUX); }
__device__ __forceinline__ void st128(rsrc_t r, int voff, v4u v) { __builtin_amdgcn_raw_buffer_store_b128(v, r, voff, 0, AUX_SC1); }
__device__ __forceinline__ void st64(rsrc_t r, int voff, v2u v) { __builtin_amdgcn_raw_buffer_store_b64(v, r, voff, 0, AUX_SC1); }
__device__ __forceinline__ v2u trread(LAS unsigned char* p) { return __builtin_bit_cast(v2u, __builtin_amdgcn_ds_read_tr16_b64_v4i16((LAS v4s*)p)); }
#define SB() __builtin_amdgcn_sched_barrier(0)
#define CB() asm volatile("" ::: "memory")
#define BAR() do { SB(); asm volatile("s_waitcnt lgkmcnt(0)" ::: "memory"); __builtin_amdgcn_s_barrier(); asm volatile("" ::: "memory"); SB(); } while (0)
__device__ __forceinline__ int swap23(int k) { return (k & ~12) | ((k & 4) << 1) | ((k & 8) >> 1); }

#define STAMP(n) do {} while (0)

#define GL(R, K, ST) "buffer_load_dwordx4 v[" #R "+4*" #K ":" #R "+4*" #K "+3], %[o], %[rs], 0 offen offset:" #K "*" #ST " sc1\n\t"
#define GC(R, K, A, B) "v_cmp_eq_u32 %[t], %[tag], v[" #R "+4*" #K "+1]\n\ts_and_b64 vcc, vcc, %[t]\n\tv_cmp_eq_u32 %[t], %[tag], v[" #R "+4*" #K "+3]\n\ts_and_b64 vcc, vcc, %[t]\n\t" \
                       "v_mov_b32 %[" #A "], v[" #R "+4*" #K "]\n\tv_mov_b32 %[" #B "], v[" #R "+4*" #K "+2]\n\t"
#define GEND "s_andn2_b64 exec, exec, vcc\n\ts_cbranch_scc0 9f\n\t"
#define GBODY(LOAD, CHECK, NV, NH, SL) \
    "s_mov_b64 %[sv], exec\n\ts_mov_b64 exec, %[act]\n\ts_mov_b32 %[cnt], 0x100000\n" \
    "1:\n\t" \
    LOAD(208) "s_mov_b64 vcc, exec\n\ts_waitcnt vmcnt(0)\n\t" CHECK(208) GEND "s_sleep 1\n\t" \
    "s_sub_u32 %[cnt], %[cnt], 1\n\ts_cbranch_scc0 1b\n" \
    "9:\n\ts_mov_b64 exec, %[sv]\n\ts_nop 1"
#define GCLOB "vcc", "scc", "memory", "v208", "v209", "v210", "v211", "v212", "v213", "v214", "v215", "v216", "v217", "v218", "v219", "v220", "v221", "v222", "v223", \
    "v224", "v225", "v226", "v227", "v228", "v229", "v230", "v231", "v232", "v233", "v234", "v235", "v236", "v237", "v238", "v239", \
    "v240", "v241", "v242", "v243", "v244", "v245", "v246", "v247", "v248", "v249", "v250", "v251", "v252", "v253", "v254", "v255"
#define GATHER1(SL, rsd, off, tagv, actv, A0, B0) do { u64 sv_, t_; unsigned cnt_; \
    asm volatile(GBODY(G1L, G1C, 2, 1, SL) : [a0] "+v"(A0), [b0] "+v"(B0), [sv] "=&s"(sv_), [t] "=&s"(t_), [cnt] "=&s"(cnt_) \
      : [o] "v"(off), [rs] "s"(rsd), [tag] "s"(tagv), [act] "s"(actv) : GCLOB); } while (0)
#define G1L(R) GL(R, 0, 0)
#define G1C(R) GC(R, 0, a0, b0)
#define G2AL(R) GL(R, 0, 1024) GL(R, 1, 1024)
#define G2BL(R) GL(R, 0, 16) GL(R, 1, 16)
#define G2DL(R) GL(R, 0, 128) GL(R, 1, 128)
#define G2C(R) GC(R, 0, a0, b0) GC(R, 1, a1, b1)
#define G4AL(R) GL(R, 0, 1024) GL(R, 1, 1024) GL(R, 2, 1024) GL(R, 3, 1024)
#define G4BL(R) GL(R, 0, 896) GL(R, 1, 896) GL(R, 2, 896) GL(R, 3, 896)
#define G4C(R) GC(R, 0, a0, b0) GC(R, 1, a1, b1) GC(R, 2, a2, b2) GC(R, 3, a3, b3)
#define GATHER2(LOADM, SL, rsd, off, tagv, actv, D) do { u64 sv_, t_; unsigned cnt_; \
    asm volatile(GBODY(LOADM, G2C, 4, 2, SL) : [a0] "+v"(D[0]), [b0] "+v"(D[1]), [a1] "+v"(D[2]), [b1] "+v"(D[3]), [sv] "=&s"(sv_), [t] "=&s"(t_), [cnt] "=&s"(cnt_) \
      : [o] "v"(off), [rs] "s"(rsd), [tag] "s"(tagv), [act] "s"(actv) : GCLOB); } while (0)
#define GATHER4(LOADM, SL, rsd, off, tagv, actv, D) do { u64 sv_, t_; unsigned cnt_; \
    asm volatile(GBODY(LOADM, G4C, 8, 4, SL) : [a0] "+v"(D[0]), [b0] "+v"(D[1]), [a1] "+v"(D[2]), [b1] "+v"(D[3]), [a2] "+v"(D[4]), [b2] "+v"(D[5]), [a3] "+v"(D[6]), [b3] "+v"(D[7]), \
      [sv] "=&s"(sv_), [t] "=&s"(t_), [cnt] "=&s"(cnt_) \
      : [o] "v"(off), [rs] "s"(rsd), [tag] "s"(tagv), [act] "s"(actv) : GCLOB); } while (0)

#define GL2(R, K) "buffer_load_dwordx4 v[" #R "+4*" #K ":" #R "+4*" #K "+3], %[o2], %[rs], 0 offen sc1\n\t"
#define G3L(R) GL(R, 0, 128) GL(R, 1, 128) GL2(R, 2)
#define G3C(R) GC(R, 0, a0, b0) GC(R, 1, a1, b1) "v_cmp_eq_u32 %[t], %[tag], v[" #R "+9]\n\ts_and_b64 vcc, vcc, %[t]\n\tv_mov_b32 %[a2], v[" #R "+8]\n\t"
#define GATHER3(SL, rsd, off, off2, tagv, actv, D) do { u64 sv_, t_; unsigned cnt_; \
    asm volatile(GBODY(G3L, G3C, 6, 3, SL) : [a0] "+v"(D[0]), [b0] "+v"(D[1]), [a1] "+v"(D[2]), [b1] "+v"(D[3]), [a2] "+v"(D[4]), [sv] "=&s"(sv_), [t] "=&s"(t_), [cnt] "=&s"(cnt_) \
      : [o] "v"(off), [o2] "v"(off2), [rs] "s"(rsd), [tag] "s"(tagv), [act] "s"(actv) : GCLOB); } while (0)
#define GLS(R, K, OFFS, SOFF) "buffer_load_dwordx4 v[" #R "+4*" #K ":" #R "+4*" #K "+3], %[o], %[rs], " SOFF " offen offset:" #OFFS " sc1\n\t"
#define G4CL(R) GLS(R, 0, 0, "0") GLS(R, 1, 2048, "0") GLS(R, 2, 0, "%[so]") GLS(R, 3, 2048, "%[so]")
#define G4DL(R) GLS(R, 0, 0, "0") GLS(R, 1, 1792, "0") GLS(R, 2, 0, "%[so]") GLS(R, 3, 1792, "%[so]")
#define GATHER4S(LOADM, SL, rsd, off, sov, tagv, actv, D) do { u64 sv_, t_; unsigned cnt_; \
    asm volatile(GBODY(LOADM, G4C, 8, 4, SL) : [a0] "+v"(D[0]), [b0] "+v"(D[1]), [a1] "+v"(D[2]), [b1] "+v"(D[3]), [a2] "+v"(D[4]), [b2] "+v"(D[5]), [a3] "+v"(D[6]), [b3] "+v"(D[7]), \
      [sv] "=&s"(sv_), [t] "=&s"(t_), [cnt] "=&s"(cnt_) \
      : [o] "v"(off), [rs] "s"(rsd), [so] "s"(sov), [tag] "s"(tagv), [act] "s"(actv) : GCLOB); } while (0)
#define GATHER4SX(a, b, c, d, e, f, g, h) GATHER4S(a, b, c, d, e, f, g, h)
#define GATHER4X(a, b, c, d, e, f, g) GATHER4(a, b, c, d, e, f, g)
__device__ __forceinline__ v4u ln_row(v4u in) {
  float v[8];
#pragma unroll
  for (int k = 0; k < 4; ++k) { v[2 * k] = lo16(in[k]); v[2 * k + 1] = hi16(in[k]); }
  const float m = xrow_sum(row16_sum(((v[0] + v[1]) + (v[2] + v[3])) + ((v[4] + v[5]) + (v[6] + v[7])))) * (1.f / 512.f);
  float q = 0.f;
#pragma unroll
  for (int j = 0; j < 8; ++j) { const float d = v[j] - m; q = fmaf(d, d, q); }
  const float var = xrow_sum(row16_sum(q)) * (1.f / 512.f);
  const float mr = rbf(m);
  const float den = rbf(__builtin_sqrtf(rbf(rbf(var) + 1e-5f)));
  const float inv = 1.f / den;
  v4u o;
#pragma unroll
  for (int k = 0; k < 4; ++k) {
    const unsigned d = pk(v[2 * k] - mr, v[2 * k + 1] - mr);
    o[k] = pk(lo16(d) * inv, hi16(d) * inv);
  }
  return o;
}

__device__ __forceinline__ void tr8(const v4u* in, v4u (&out)[8]) {
#pragma unroll
  for (int m = 0; m < 4; ++m)
#pragma unroll
    for (int jp = 0; jp < 4; ++jp) {
      out[2 * m][jp] = __builtin_amdgcn_perm(in[jp + 4][m], in[jp][m], 0x05040100u);
      out[2 * m + 1][jp] = __builtin_amdgcn_perm(in[jp + 4][m], in[jp][m], 0x07060302u);
    }
}

template <int SLEEP = 0> __device__ __forceinline__ void lds_wait4(LAS unsigned char* p) {
  for (unsigned spins = 0; spins < (SPIN_CAP << 4); ++spins) {
    const v4u f = *(volatile LAS v4u*)p;
    if (__builtin_amdgcn_readfirstlane(f.x & f.y & f.z & f.w) != 0) break;
    if (SLEEP) __builtin_amdgcn_s_sleep(SLEEP);
  }
  CB();
}

#define SETFLAG(F) do { CB(); *(volatile LAS unsigned*)(L + (F) + w * 4) = 1u; } while (0)

__global__ void __launch_bounds__(512, 2)
fused_kernel(const u16* __restrict__ x, const u16* __restrict__ kc, const u16* __restrict__ vc, const u16* __restrict__ wq,
             const u16* __restrict__ wo, const u16* __restrict__ w1, const u16* __restrict__ b1, const u16* __restrict__ w2,
             const u16* __restrict__ b2, u16* __restrict__ out, unsigned char* __restrict__ ws) {
  __shared__ __attribute__((aligned(16))) unsigned char smem[LDS_BYTES];
  LAS unsigned char* L = (LAS unsigned char*)smem;
  STAMP(0);
  const int tid = threadIdx.x, lane = tid & 63, wave = __builtin_amdgcn_readfirstlane(tid >> 6);
  const int wg = blockIdx.x, kvh = wg & 7, r = wg >> 3, b = r >> 3, s = r & 7;
  const int l16 = lane & 15, c = lane >> 4;
  const int ks = kvh * 8 + (r >> 2), ns = r & 3;
  const int ks2 = kvh * 7 + (r >> 2);
  const bool has2 = r < 28;
  if (tid < 44) *(LAS unsigned*)(L + L_FLAG + tid * 4) = 0u;
  unsigned pf = 0;
  if (wave == 3) {
    u64 pc; unsigned len;
    asm volatile("s_getpc_b64 %0\n.Lpc_here:\n\ts_mov_b32 %1, .Lk_end-.Lpc_here" : "=s"(pc), "=s"(len));
    pf = ld32<PFAUX>(mkrs((const void*)pc, len), ((r + PFROT) & 31) * 512 + lane * 8, 0);
  }
  asm volatile("" :: "s"(x), "s"(kc), "s"(vc), "s"(wq), "s"(wo), "s"(w1), "s"(b1), "s"(w2), "s"(b2), "s"(out), "s"(ws));
  BAR();
  const rsrc_t rws = mkrs(ws, WS_BYTES);
  const unsigned ep = *(const __attribute__((address_space(4))) unsigned*)(ws + EP_OFF);
  unsigned tag;
#define MAKE_TAG() asm volatile("s_add_u32 %0, %1, 1" : "=s"(tag) : "s"(ep) : "scc")
  const int w = wave & 3;
  const size_t kvbase = ((size_t)(b * 8 + kvh) * 8192 + 6144) * 128;
  int tso[4];
#pragma unroll
  for (int t = 0; t < 4; ++t) tso[t] = (32 * t + ((4 * s + w + ROT * t) & 31)) * 4096;
  const int dup = l16 & 3;
  const bool has1 = (r >> 2) != 3;
  const int u1 = kvh * 28 + (r < 12 ? r : r - 4);
  const int kq = r & 3, ke = u1 & 7;

  v4u xr, wv[4], uv[2], K[16];
  const rsrc_t rk = mkrs(kc + kvbase, 2048 * 256);
  const int koff = (l16 >> 2) * 256 + c * 64 + dup * 16;
  if (wave >= 4) {
    __builtin_amdgcn_s_setprio(VPRIO);
    const rsrc_t rwq = mkrs(wq + kvh * 512 + (r >> 2) * 64, (EXP & 2) ? 0 : 512 * 8192);
    const rsrc_t rw1 = mkrs(w1 + (u1 >> 3) * 64, (has1 && !(EXP & 1)) ? 512 * 3584 : 0);
    xr = ld128<0>(mkrs(x, 4096), w * 1024 + lane * 16, 0);
    SB();
#pragma unroll
    for (int n = 0; n < 4; ++n) wv[n] = ld128<AUX_NT>(rwq, (lane >> 3) * 8192 + (lane & 7) * 16, (kq * 128 + w * 32 + n * 8) * 8192);
    SB();
#pragma unroll
    for (int n = 0; n < 2; ++n) uv[n] = ld128<AUX_NT>(rw1, (lane >> 3) * 3584 + (lane & 7) * 16, (ke * 64 + w * 16 + n * 8) * 3584);
    SB();
  } else {
    __builtin_amdgcn_s_sleep(KDELAY);
#pragma unroll
    for (int n = 0; n < WK; ++n) K[n] = ld128<AUX_NT>(rk, koff + (n & 3) * 1024, tso[n >> 2]);
    SB();
  }
  int wave2 = wave;
  asm volatile("" : "+s"(wave2));

  if (wave2 < 4) {
    const int qoff = QP_OFF + ((kvh * 4 + b) * 4 + w) * 4096 + lane * 16;
    v4u Q[4];
#pragma unroll
    for (int n = WK; n < 16; ++n) {
      asm volatile("s_waitcnt vmcnt(%0)" :: "n"(WK - 1) : "memory");
      K[n] = ld128<AUX_NT>(rk, koff + (n & 3) * 1024, tso[n >> 2]);
      SB();
    }
    SETFLAG(F_K);
    asm volatile("s_waitcnt vmcnt(%0)" :: "n"(QW) : "memory");
#pragma unroll
    for (int k = 0; k < 4; ++k) Q[k] = ld128<AUX_SC1>(rws, qoff + k * 1024, 0);
    const int oidx = (wg >> 6) * 512 + (wg & 7) * 64 + ((wg >> 5) & 1) * 32 + ((wg >> 3) & 3) * 4 + ((lane >> 1) & 1) * 16 + (lane & 1) * 2;
    const unsigned xw = ld32<0>(mkrs(x, 4096), oidx * 2, 0);
    const unsigned bw = ld32<0>(mkrs(b2, 1024), (oidx & 511) * 2, 0);
    SB();
    MAKE_TAG();
    __builtin_amdgcn_s_setprio(KPRIO);
    asm volatile("" :: "v"(pf));
    STAMP(1);
    {
      float qd[8];
      unsigned bad = 0;
#pragma unroll
      for (int k = 0; k < 4; ++k) { qd[2 * k] = __uint_as_float(Q[k].x); qd[2 * k + 1] = __uint_as_float(Q[k].z); bad |= (Q[k].y ^ tag) | (Q[k].w ^ tag); }
      const u64 miss = __builtin_amdgcn_ballot_w64(bad != 0);
      if (miss) GATHER4X(G4AL, QSL, rws, qoff, tag, miss, qd);
      *(LAS unsigned*)(L + L_Q + w * 256 + lane * 4) = pk((qd[0] + qd[2]) + (qd[4] + qd[6]), (qd[1] + qd[3]) + (qd[5] + qd[7]));
      SETFLAG(F_Q);
    }
    v4u Z[8];
    unsigned bias = 0;
    const int zoff = has2 ? ZP_OFF + ks2 * 8192 + lane * 16 : OOB - 8192;
    if (wave2 == 3) {
#pragma unroll
      for (int k = 0; k < 8; ++k) Z[k] = ld128<AUX_SC1>(rws, zoff + (k & 3) * 1024, (k >> 2) * 4096);
      bias = ld32<0>(mkrs(b1, 3584), (ks2 * 32 + l16 * 2) * 2, 0);
      SB();
    }
    STAMP(2);
    lds_wait4(L + F_Q);
    STAMP(13);
    const v4u qf = *(LAS v4u*)(L + L_Q + dup * 256 + c * 64 + (l16 >> 2) * 16);
    float sc[4];
#pragma unroll
    for (int mm = 0; mm < 4; ++mm) {
      float t[4];
#pragma unroll
      for (int be = 0; be < 4; ++be) {
        const f32x4 d = mfma(K[4 * mm + be], qf, f32x4{0.f, 0.f, 0.f, 0.f});
        t[be] = ((d[be] + dpp<0x12C>(d[(be + 1) & 3])) + dpp<0x128>(d[(be + 2) & 3])) + dpp<0x124>(d[(be + 3) & 3]);
      }
      const float t01 = (l16 & 4) ? t[1] : t[0], t23 = (l16 & 4) ? t[3] : t[2];
      sc[mm] = (l16 & 8) ? t23 : t01;
    }
    {
      const unsigned d0 = pk(sc[0], sc[1]), d1 = pk(sc[2], sc[3]);
      const unsigned e0 = pk(lo16(d0) * 0.08838834764831845f, hi16(d0) * 0.08838834764831845f);
      const unsigned e1 = pk(lo16(d1) * 0.08838834764831845f, hi16(d1) * 0.08838834764831845f);
      sc[0] = lo16(e0); sc[1] = hi16(e0); sc[2] = lo16(e1); sc[3] = hi16(e1);
    }
    float m = fmaxf(fmaxf(sc[0], sc[1]), fmaxf(sc[2], sc[3]));
    m = fmaxf(m, dpp<0x124>(m)); m = fmaxf(m, dpp<0x128>(m));
    m = xrow_max(m);
    const float p0 = ex2((sc[0] - m) * LOG2E), p1 = ex2((sc[1] - m) * LOG2E), p2 = ex2((sc[2] - m) * LOG2E), p3 = ex2((sc[3] - m) * LOG2E);
    *(LAS unsigned*)(L + L_P + w * 512 + dup * 64 + c * 16 + (l16 >> 2) * 4) = pk(p0, p1);
    *(LAS unsigned*)(L + L_P + w * 512 + 256 + dup * 64 + c * 16 + (l16 >> 2) * 4) = pk(p2, p3);
    float l = (p0 + p1) + (p2 + p3);
    l += dpp<0x124>(l); l += dpp<0x128>(l);
    l = xrow_sum(l);
    if (lane < 4) *(LAS f32x2*)(L + L_ST + lane * 32 + w * 8) = f32x2{m, l};
    SETFLAG(F_P);
    STAMP(3);
    if (wave2 == 3) {
      float zd[8], ze[8];
      unsigned bad = 0;
#pragma unroll
      for (int k = 0; k < 4; ++k) {
        zd[2 * k] = __uint_as_float(Z[k].x); zd[2 * k + 1] = __uint_as_float(Z[k].z); bad |= (Z[k].y ^ tag) | (Z[k].w ^ tag);
        ze[2 * k] = __uint_as_float(Z[4 + k].x); ze[2 * k + 1] = __uint_as_float(Z[4 + k].z); bad |= (Z[4 + k].y ^ tag) | (Z[4 + k].w ^ tag);
      }
      const u64 miss = __builtin_amdgcn_ballot_w64(bad != 0);
      if (has2 && miss) { GATHER4(G4AL, 8, rws, zoff, tag, miss, zd); GATHER4(G4AL, 8, rws, zoff + 4096, tag, miss, ze); }
      const unsigned zp = pk(((zd[0] + zd[2]) + (zd[4] + zd[6])) + ((ze[0] + ze[2]) + (ze[4] + ze[6])), ((zd[1] + zd[3]) + (zd[5] + zd[7])) + ((ze[1] + ze[3]) + (ze[5] + ze[7])));
      const unsigned zb = pk(lo16(zp) + lo16(bias), hi16(zp) + hi16(bias));
      float z[2] = {lo16(zb), hi16(zb)};
#pragma unroll
      for (int i = 0; i < 2; ++i) {
        const float zz = z[i];
        const float u = 0.7978845608028654f * (zz + 0.044715f * zz * zz * zz);
        const float th = 1.f - 2.f * rcp(ex2(u * (2.f * LOG2E)) + 1.f);
        z[i] = 0.5f * zz * (1.f + th);
      }
      *(LAS unsigned*)(L + L_H + lane * 4) = pk(z[0], z[1]);
      CB();
      if (lane < 4) *(volatile LAS unsigned*)(L + F_H + lane * 4) = 1u;
    }
    STAMP(4);
    const int sp = lane & 7, k4 = lane >> 3;
    const int osw = L_OS + w * OS_STRIDE + k4 * 8;
    const int p1g = P1_OFF + (ks * 32 + ((w * 8 + sp + SKEW * ks) & 31)) * 256 + k4 * 16;
    const int l1g = L1_OFF + (((ks >> 1) * 4 + w) * 8 + sp) * 128;
    asm volatile("" :: "v"(osw), "v"(l1g), "v"(p1g), "v"(xw), "v"(bw));
    lds_wait4<2>(L + F_O);
    __builtin_amdgcn_s_sleep(PSL);
    STAMP(12);
    unsigned d[5] = {0u, 0u, 0u, 0u, 0u};
    GATHER3(4, rws, p1g, l1g, tag, ~0ull, d);
    STAMP(6);
    {
      const float lse = __uint_as_float(d[4]);
      const float e = ex2(lse - oct_max(lse));
      const float inv = rcp(oct_sum(e));
      float a[8];
#pragma unroll
      for (int k = 0; k < 4; ++k) { a[2 * k] = oct_sum(e * lo16(d[k])) * inv; a[2 * k + 1] = oct_sum(e * hi16(d[k])) * inv; }
      if (sp == 0) {
        *(LAS v2u*)(L + osw) = v2u{pk(a[0], a[1]), pk(a[2], a[3])};
        *(LAS v2u*)(L + osw + 64) = v2u{pk(a[4], a[5]), pk(a[6], a[7])};
      }
      SETFLAG(F_OS);
    }
    const int foff = ((wg >> 7) * 64 + (wg & 63)) * 8192 + ((wg >> 6) & 1) * 64 + (lane >> 2) * 128 + (lane & 3) * 16;
    if (wave2 == 1) {
      float dg[8] = {0.f, 0.f, 0.f, 0.f, 0.f, 0.f, 0.f, 0.f};
      GATHER4SX(G4DL, GPS, rws, G2_OFF + foff, 3584, tag, (1ull << 56) - 1ull, dg);
      float g0 = (dg[0] + dg[2]) + (dg[4] + dg[6]);
      float g1 = (dg[1] + dg[3]) + (dg[5] + dg[7]);
      g0 += dpp<0x124>(g0); g1 += dpp<0x124>(g1);
      g0 += dpp<0x128>(g0); g1 += dpp<0x128>(g1);
      g0 = xrow_sum(g0); g1 = xrow_sum(g1);
      const unsigned gp = pk(g0, g1);
      const unsigned gb = pk(lo16(gp) + lo16(bw), hi16(gp) + hi16(bw));
      const float z0 = lo16(gb), z1 = hi16(gb);
      const unsigned sg = pk(z0 * rcp(1.f + ex2(-z0 * LOG2E)), z1 * rcp(1.f + ex2(-z1 * LOG2E)));
      if (lane < 4) *(volatile LAS v2u*)(L + F_G + lane * 8) = v2u{sg, 1u};
      STAMP(8);
    } else if (wave2 == 0) {
      __builtin_amdgcn_s_sleep(FSL);
      float ds[8] = {0.f, 0.f, 0.f, 0.f, 0.f, 0.f, 0.f, 0.f};
      GATHER4SX(G4CL, FPS, rws, P2_OFF + foff, 4096, tag, ~0ull, ds);
      STAMP(9);
      st128(rws, (wg == 0 && lane < 4) ? EP_OFF + lane * 16 : OOB, v4u{tag, tag, tag, tag});
      v2u sgt = *(volatile LAS v2u*)(L + F_G + (lane & 3) * 8);
      float s0 = (ds[0] + ds[2]) + (ds[4] + ds[6]);
      float s1 = (ds[1] + ds[3]) + (ds[5] + ds[7]);
      s0 += dpp<0x124>(s0); s1 += dpp<0x124>(s1);
      s0 += dpp<0x128>(s0); s1 += dpp<0x128>(s1);
      s0 = xrow_sum(s0); s1 = xrow_sum(s1);
      const unsigned sq = pk(s0, s1);
      for (unsigned spins = 0; !__all(sgt.y != 0u) && spins < (SPIN_CAP << 4); ++spins) sgt = *(volatile LAS v2u*)(L + F_G + (lane & 3) * 8);
      const unsigned pr = pk(lo16(sq) * lo16(sgt.x), hi16(sq) * hi16(sgt.x));
      const unsigned res = pk(lo16(xw) + lo16(pr), hi16(xw) + hi16(pr));
      if (lane < 4) *(GAS unsigned*)(out + oidx) = res;
      STAMP(10);
    }
  } else {
    const v4u xn = ln_row(xr);
    *(LAS v4u*)(L + L_XN + w * XS_STRIDE + lane * 16) = xn;
    MAKE_TAG();
    STAMP(1);
    const int sw = (w * 32 + ((lane >> 5) & 1) * 8 + ((lane >> 3) & 3)) * WT_RS + (lane & 7) * 16;
#pragma unroll
    for (int n = 0; n < 4; ++n) *(LAS v4u*)(L + L_WQ + sw + ((n >> 1) * 16 + (n & 1) * 4) * WT_RS) = wv[n];
    SETFLAG(F_A);
    const int ta = ((c >> 1) * 16 + (c & 1) * 4 + (l16 >> 2)) * WT_RS + w * 32 + (l16 & 3) * 8;
    const int xb = (l16 >> 2) * XS_STRIDE + c * 16;
    lds_wait4(L + F_A);
    {
      f32x4 acc = {0.f, 0.f, 0.f, 0.f};
#pragma unroll
      for (int kk = 0; kk < 4; ++kk) {
        const v2u a0 = trread(L + L_WQ + ta + kk * 32 * WT_RS), a1 = trread(L + L_WQ + ta + (kk * 32 + 8) * WT_RS);
        acc = mfma(*(LAS v4u*)(L + L_XN + xb + kq * 256 + kk * 64), v4u{a0.x, a0.y, a1.x, a1.y}, acc);
      }
      st64(rws, QP_OFF + ((((kvh * 4 + c) * 4 + (r >> 3)) * 4 + kq) * 128 + ((r >> 2) & 1) * 64 + w * 16 + l16) * 8, v2u{__float_as_uint(acc[0]), tag});
    }
    SB();
    STAMP(2);
    const v4u yn = ln_row(xn);
    *(LAS v4u*)(L + L_YN + w * XS_STRIDE + lane * 16) = yn;
#pragma unroll
    for (int n = 0; n < 2; ++n) *(LAS v4u*)(L + L_W1 + sw + (n * 4 - w * 16) * WT_RS) = uv[n];
    SETFLAG(F_B);
    lds_wait4(L + F_B);
    {
      f32x4 acc = {0.f, 0.f, 0.f, 0.f};
#pragma unroll
      for (int kk = 0; kk < 2; ++kk) {
        const v2u a0 = trread(L + L_W1 + ta + kk * 32 * WT_RS), a1 = trread(L + L_W1 + ta + (kk * 32 + 8) * WT_RS);
        acc = mfma(*(LAS v4u*)(L + L_YN + xb + ke * 128 + kk * 64), v4u{a0.x, a0.y, a1.x, a1.y}, acc);
      }
      const int off = ZP_OFF + (((((u1 >> 3) * 2 + (w >> 1)) * 8 + ke) * 4 + c) * 32 + (w & 1) * 16 + l16) * 8;
      st64(rws, has1 ? off : OOB, v2u{__float_as_uint(acc[0]), tag});
    }
    __builtin_amdgcn_s_setprio(0);
    STAMP(3);
    for (unsigned spins = 0; spins < (SPIN_CAP << 4); ++spins) {
      if (*(volatile LAS unsigned*)(L + F_K + w * 4) != 0u) break;
      __builtin_amdgcn_s_sleep(1);
    }
    const rsrc_t rv = mkrs(vc + kvbase, 2048 * 256);
    const int voff = lane * 16;
    const rsrc_t rwo = mkrs(wo + (size_t)ks * 64 * 512 + ns * 128, 64 * 1024);
    const rsrc_t rw2 = mkrs(w2 + (size_t)ks2 * 32 * 512 + ns * 128, has2 ? 32 * 1024 : 0);
    v4u V[16], WO[6];
#pragma unroll
    for (int n = 0; n < 16; ++n) {
      if (n >= WV) asm volatile("s_waitcnt vmcnt(%0)" :: "n"(WV - 1) : "memory");
      V[n] = ld128<AUX_NT>(rv, voff + (n & 3) * 1024, tso[n >> 2]);
      SB();
      if (n < VRN && VRAMP) { __builtin_amdgcn_s_sleep(VRAMP); SB(); }
    }
    STAMP(4);
#define WO_LOAD(n) do { asm volatile("s_waitcnt vmcnt(%0)" :: "n"(WV - 1) : "memory"); \
      WO[n] = (n) < 4 ? ld128<AUX_NT>(rwo, (16 * w + c) * 1024 + l16 * 16, (n) * 4096) : ld128<AUX_NT>(rw2, (8 * w + c) * 1024 + l16 * 16, ((n) - 4) * 4096); SB(); } while (0)
    lds_wait4<1>(L + F_P);
    v4u P[2];
    P[0] = *(LAS v4u*)(L + L_P + w * 512 + (l16 >> 2) * 64 + c * 16);
    P[1] = *(LAS v4u*)(L + L_P + w * 512 + 256 + (l16 >> 2) * 64 + c * 16);
    float wgt[4];
    unsigned lse2;
    {
      const f32x4 s0 = *(LAS f32x4*)(L + L_ST + w * 32), s1 = *(LAS f32x4*)(L + L_ST + w * 32 + 16);
      const float M = fmaxf(fmaxf(s0[0], s0[2]), fmaxf(s1[0], s1[2]));
      wgt[0] = ex2((s0[0] - M) * LOG2E);
      wgt[1] = ex2((s0[2] - M) * LOG2E);
      wgt[2] = ex2((s1[0] - M) * LOG2E);
      wgt[3] = ex2((s1[2] - M) * LOG2E);
      const float Ls = wgt[0] * s0[1] + wgt[1] * s0[3] + wgt[2] * s1[1] + wgt[3] * s1[3];
      const float inv = rcp(Ls);
#pragma unroll
      for (int k = 0; k < 4; ++k) wgt[k] *= inv;
      lse2 = __float_as_uint(M * LOG2E + __builtin_amdgcn_logf(Ls));
    }
    const int ksp = (kvh * 4 + w) * 2 + (c & 1);
    const int p1off = lane < 32 ? P1_OFF + (ksp * 32 + ((r + SKEW * ksp) & 31)) * 256 + l16 * 16
                    : lane < 40 ? L1_OFF + (((kvh * 4 + w) * 4 + b) * 8 + s) * 128 + l16 * 16 : OOB;
    const int ppw = L_PART + w * 2048 + c * 512 + l16 * 32, ppr = L_PART + w * 512 + (lane & 31) * 16;
    f32x4 o[8];
    {
      v4u vt[8];
      tr8(&V[0], vt);
#pragma unroll
      for (int e = 0; e < 8; ++e) o[e] = mfma(P[0], vt[e], f32x4{0.f, 0.f, 0.f, 0.f});
    }
    asm volatile("" :: "v"(p1off), "v"(ppw), "v"(ppr), "v"(wgt[0]), "v"(wgt[1]), "v"(wgt[2]), "v"(wgt[3]), "v"(lse2));
    SB();
    asm volatile("s_waitcnt vmcnt(%0)" :: "n"(WOAT) : "memory");
#pragma unroll
    for (int n = 0; n < 4; ++n) WO[n] = ld128<AUX_NT>(rwo, (16 * w + c) * 1024 + l16 * 16, n * 4096);
#pragma unroll
    for (int n = 0; n < 2; ++n) WO[4 + n] = ld128<AUX_NT>(rw2, (8 * w + c) * 1024 + l16 * 16, n * 4096);
    SB();
    {
      v4u vt[8];
      tr8(&V[8], vt);
#pragma unroll
      for (int e = 0; e < 8; ++e) o[e] = mfma(P[1], vt[e], o[e]);
    }
    ((LAS f32x4*)(L + ppw))[0] = f32x4{o[0][0], o[1][0], o[2][0], o[3][0]};
    ((LAS f32x4*)(L + ppw))[1] = f32x4{o[4][0], o[5][0], o[6][0], o[7][0]};
    SETFLAG(F_O);
    STAMP(11);
    lds_wait4(L + F_O);
    {
      const LAS f32x4* rp = (const LAS f32x4*)(L + ppr);
      const f32x4 a = (wgt[0] * rp[0] + wgt[1] * rp[128]) + (wgt[2] * rp[256] + wgt[3] * rp[384]);
      st128(rws, p1off, v4u{lane < 32 ? pk(a[0], a[1]) : lse2, tag, pk(a[2], a[3]), tag});
    }
    STAMP(5);
#pragma unroll
    for (int n = 0; n < 4; ++n) *(LAS v4u*)(L + L_WO + swap23(16 * w + 4 * n + c) * WO_RS + l16 * 16) = WO[n];
#pragma unroll
    for (int n = 0; n < 2; ++n) *(LAS v4u*)(L + L_WO + (64 + swap23(8 * w + 4 * n + c)) * WO_RS + l16 * 16) = WO[4 + n];
    SETFLAG(F_WO);
    STAMP(14);
    const int fpair = (ns * 2 + (w >> 1)) + 8 * ((l16 >> 3) * 8 + (w & 1) * 4 + c);
    const int sk = ks + SKEW * (fpair >> 3);
    const int p2off = P2_OFF + (fpair * 64 + (sk & 63)) * 128 + (l16 & 7) * 16;
    const int sk2 = ks2 + SKEW * (fpair >> 3);
    const int g2off = G2_OFF + (fpair * 64 + (sk2 >= 56 ? sk2 - 56 : sk2)) * 128 + (l16 & 7) * 16;
    const int osr = L_OS + (l16 >> 2) * OS_STRIDE + c * 16;
    lds_wait4(L + F_WO);
    v4u af[2][2];
    {
      LAS unsigned char* tw = L + L_WO + ((c >> 1) * 16 + (c & 1) * 4 + (l16 >> 2)) * WO_RS + w * 64 + (l16 & 3) * 8;
#pragma unroll
      for (int ct = 0; ct < 2; ++ct)
#pragma unroll
        for (int kk = 0; kk < 2; ++kk) {
          const v2u a0 = trread(tw + ct * 32 + kk * 32 * WO_RS), a1 = trread(tw + ct * 32 + kk * 32 * WO_RS + 8 * WO_RS);
          af[ct][kk] = v4u{a0.x, a0.y, a1.x, a1.y};
        }
      lds_wait4<1>(L + F_H);
      const v4u hb = *(LAS v4u*)(L + L_H + (l16 >> 2) * 64 + c * 16);
      f32x4 g[2];
#pragma unroll
      for (int ct = 0; ct < 2; ++ct) {
        const v2u a0 = trread(tw + ct * 32 + 64 * WO_RS), a1 = trread(tw + ct * 32 + 72 * WO_RS);
        g[ct] = mfma(v4u{a0.x, a0.y, a1.x, a1.y}, hb, f32x4{0.f, 0.f, 0.f, 0.f});
      }
      const f32x4 acc = (dup & 2) ? g[1] : g[0];
      const float v0 = (dup & 1) ? acc[2] : acc[0], v1 = (dup & 1) ? acc[3] : acc[1];
      st128(rws, has2 ? g2off : OOB, v4u{__float_as_uint(v0), tag, __float_as_uint(v1), tag});
    }
    asm volatile("" :: "v"(p2off), "v"(osr), "v"(af[0][0]), "v"(af[0][1]), "v"(af[1][0]), "v"(af[1][1]));
    STAMP(6);
    lds_wait4(L + F_OS);
    {
      const v4u b0 = *(LAS v4u*)(L + osr), b1f = *(LAS v4u*)(L + osr + 64);
      f32x4 acc0 = mfma(af[0][0], b0, f32x4{0.f, 0.f, 0.f, 0.f});
      f32x4 acc1 = mfma(af[1][0], b0, f32x4{0.f, 0.f, 0.f, 0.f});
      acc0 = mfma(af[0][1], b1f, acc0);
      acc1 = mfma(af[1][1], b1f, acc1);
      const f32x4 acc = (dup & 2) ? acc1 : acc0;
      const float v0 = (dup & 1) ? acc[2] : acc[0], v1 = (dup & 1) ? acc[3] : acc[1];
      st128(rws, p2off, v4u{__float_as_uint(v0), tag, __float_as_uint(v1), tag});
    }
    STAMP(7);
  }
  asm volatile(".Lk_end:");
}

extern "C" void kernel_launch(void* const* d_in, const int* in_sizes, int n_in,
                              void* d_out, int out_size, void* d_ws, size_t ws_size,
                              hipStream_t stream) {
  fused_kernel<<<256, 512, 0, stream>>>((const u16*)d_in[0], (const u16*)d_in[1], (const u16*)d_in[2], (const u16*)d_in[3],
                                        (const u16*)d_in[4], (const u16*)d_in[6], (const u16*)d_in[7], (const u16*)d_in[8],
                                        (const u16*)d_in[9], (u16*)d_out, (unsigned char*)d_ws);
}
```

```cpp
#include <hip/hip_runtime.h>
#include <stdint.h>
#define PADW 0
#define PADN -1

#define GAS __attribute__((address_space(1)))
#define LAS __attribute__((address_space(3)))
typedef unsigned short u16;
typedef unsigned long long u64;
typedef unsigned v4u __attribute__((ext_vector_type(4)));
typedef unsigned v2u __attribute__((ext_vector_type(2)));
typedef short v4s __attribute__((ext_vector_type(4)));
typedef float f32x4 __attribute__((ext_vector_type(4)));
typedef float f32x2 __attribute__((ext_vector_type(2)));
typedef __bf16 v8bf __attribute__((ext_vector_type(8)));
typedef __bf16 v2bf __attribute__((ext_vector_type(2)));
typedef __amdgpu_buffer_rsrc_t rsrc_t;

constexpr int AUX_NT = 2, AUX_SC1 = 16;
constexpr unsigned SPIN_CAP = 1u << 20;
constexpr int OOB = 0x7ffffff0;
constexpr float LOG2E = 1.4426950408889634f;

#define EXP 0
#define QSL 8
#define QAT 15
#define VWAITK 1
#define FSL 30
#define KDELAY 6
#define GSL 20
#define VPRIO 3
#define LSL 16
#define PSL 12
#define VRAMP 5
#define ROT 9
#define SKEW 2
#define KPRIO 2
#define GPS 4
#define FPS 3
#define GMODE 1
#define WK 6
#define WV 6
constexpr int EP_OFF = 0;
constexpr int QP_OFF = 4096;
constexpr int ZP_OFF = QP_OFF + 524288;
constexpr int L1_OFF = ZP_OFF + 229376;
constexpr int P1_OFF = L1_OFF + 131072;
constexpr int P2_OFF = P1_OFF + 524288;
constexpr int G2_OFF = P2_OFF + 1048576;
constexpr int WS_BYTES = G2_OFF + 1048576;
constexpr int ST_OFF = 8 << 20;

constexpr int XS_STRIDE = 1024 + 64;
constexpr int WT_RS = 160;
constexpr int L_WQ = 0;
constexpr int L_W1 = L_WQ + 128 * WT_RS;
constexpr int L_XN = L_W1 + 128 * WT_RS;
constexpr int L_YN = L_XN + 4 * XS_STRIDE;
constexpr int L_Q = L_YN + 4 * XS_STRIDE;
constexpr int L_ST = L_Q + 1024;
constexpr int L_FLAG = L_ST + 128;
constexpr int F_A = L_FLAG, F_B = L_FLAG + 16, F_Q = L_FLAG + 32, F_K = L_FLAG + 48, F_P = L_FLAG + 64, F_O = L_FLAG + 80, F_WO = L_FLAG + 96, F_OS = L_FLAG + 112, F_G = L_FLAG + 128, F_H = L_FLAG + 160;
constexpr int L_P = L_FLAG + 176;
constexpr int L_PART = L_P + 2048;
constexpr int WO_RS = 288;
constexpr int L_WO = L_PART + 8192;
constexpr int OS_STRIDE = 128 + 16;
constexpr int L_OS = L_WO + 96 * WO_RS;
constexpr int L_H = L_OS + 4 * OS_STRIDE;
constexpr int LDS_BYTES = L_H + 256;

__device__ __forceinline__ unsigned pk(float lo, float hi) { f32x2 v = {lo, hi}; return __builtin_bit_cast(unsigned, __builtin_convertvector(v, v2bf)); }
__device__ __forceinline__ float lo16(unsigned u) { return __builtin_bit_cast(float, u << 16); }
__device__ __forceinline__ float hi16(unsigned u) { return __builtin_bit_cast(float, u & 0xffff0000u); }
__device__ __forceinline__ float rbf(float x) { return lo16(pk(x, x)); }
__device__ __forceinline__ float rcp(float x) { return __builtin_amdgcn_rcpf(x); }
__device__ __forceinline__ float ex2(float x) { return __builtin_amdgcn_exp2f(x); }
template <int CTRL> __device__ __forceinline__ float dpp(float x) {
  return __builtin_bit_cast(float, __builtin_amdgcn_mov_dpp(__builtin_bit_cast(int, x), CTRL, 0xf, 0xf, true));
}
__device__ __forceinline__ float row16_sum(float x) { x += dpp<0x128>(x); x += dpp<0x124>(x); x += dpp<0x122>(x); x += dpp<0x121>(x); return x; }
__device__ __forceinline__ float oct_sum(float x) { x += dpp<0xB1>(x); x += dpp<0x4E>(x); x += dpp<0x141>(x); return x; }
__device__ __forceinline__ float oct_max(float x) { x = fmaxf(x, dpp<0xB1>(x)); x = fmaxf(x, dpp<0x4E>(x)); x = fmaxf(x, dpp<0x141>(x)); return x; }
__device__ __forceinline__ float xrow_max(float x) {
  auto s = __builtin_amdgcn_permlane16_swap(__float_as_uint(x), __float_as_uint(x), false, false);
  x = fmaxf(__uint_as_float(s[0]), __uint_as_float(s[1]));
  auto t = __builtin_amdgcn_permlane32_swap(__float_as_uint(x), __float_as_uint(x), false, false);
  return fmaxf(__uint_as_float(t[0]), __uint_as_float(t[1]));
}
__device__ __forceinline__ float xrow_sum(float x) {
  auto s = __builtin_amdgcn_permlane16_swap(__float_as_uint(x), __float_as_uint(x), false, false);
  x = __uint_as_float(s[0]) + __uint_as_float(s[1]);
  auto t = __builtin_amdgcn_permlane32_swap(__float_as_uint(x), __float_as_uint(x), false, false);
  return __uint_as_float(t[0]) + __uint_as_float(t[1]);
}
__device__ __forceinline__ f32x4 mfma(v4u a, v4u b, f32x4 c) {
  return __builtin_amdgcn_mfma_f32_16x16x32_bf16(__builtin_bit_cast(v8bf, a), __builtin_bit_cast(v8bf, b), c, 0, 0, 0);
}
__device__ __forceinline__ rsrc_t mkrs(const void* p, int bytes) { return __builtin_amdgcn_make_buffer_rsrc((void*)p, 0, bytes, 0x00020000); }
template <int AUX> __device__ __forceinline__ v4u ld128(rsrc_t r, int voff, int soff) { return __builtin_amdgcn_raw_buffer_load_b128(r, voff, soff, AUX); }
template <int AUX> __device__ __forceinline__ v2u ld64(rsrc_t r, int voff, int soff) { return __builtin_amdgcn_raw_buffer_load_b64(r, voff, soff, AUX); }
template <int AUX> __device__ __forceinline__ unsigned ld32(rsrc_t r, int voff, int soff) { return __builtin_amdgcn_raw_buffer_load_b32(r, voff, soff, AUX); }
__device__ __forceinline__ void st128(rsrc_t r, int voff, v4u v) { __builtin_amdgcn_raw_buffer_store_b128(v, r, voff, 0, AUX_SC1); }
__device__ __forceinline__ void st64(rsrc_t r, int voff, v2u v) { __builtin_amdgcn_raw_buffer_store_b64(v, r, voff, 0, AUX_SC1); }
__device__ __forceinline__ v2u trread(LAS unsigned char* p) { return __builtin_bit_cast(v2u, __builtin_amdgcn_ds_read_tr16_b64_v4i16((LAS v4s*)p)); }
#define SB() __builtin_amdgcn_sched_barrier(0)
#define CB() asm volatile("" ::: "memory")
#define BAR() do { SB(); asm volatile("s_waitcnt lgkmcnt(0)" ::: "memory"); __builtin_amdgcn_s_barrier(); asm volatile("" ::: "memory"); SB(); } while (0)
__device__ __forceinline__ int swap23(int k) { return (k & ~12) | ((k & 4) << 1) | ((k & 8) >> 1); }

#define STAMP(n) do { if ((n) == PADN) { if (__builtin_amdgcn_readfirstlane(threadIdx.x >> 6) == PADW) { u64 t_; SB(); asm volatile("s_memrealtime %0\n\ts_waitcnt lgkmcnt(0)" : "=s"(t_) :: "memory"); SB(); *(volatile LAS u64*)(L + LDS_BYTES) = t_; } } } while (0)

#define GL(R, K, ST) "buffer_load_dwordx4 v[" #R "+4*" #K ":" #R "+4*" #K "+3], %[o], %[rs], 0 offen offset:" #K "*" #ST " sc1\n\t"
#define GC(R, K, A, B) "v_cmp_eq_u32 %[t], %[tag], v[" #R "+4*" #K "+1]\n\ts_and_b64 vcc, vcc, %[t]\n\tv_cmp_eq_u32 %[t], %[tag], v[" #R "+4*" #K "+3]\n\ts_and_b64 vcc, vcc, %[t]\n\t" \
                       "v_mov_b32 %[" #A "], v[" #R "+4*" #K "]\n\tv_mov_b32 %[" #B "], v[" #R "+4*" #K "+2]\n\t"
#define GEND "s_andn2_b64 exec, exec, vcc\n\ts_cbranch_scc0 9f\n\t"
#define GBODY(LOAD, CHECK, NV, NH, SL) \
    "s_mov_b64 %[sv], exec\n\ts_mov_b64 exec, %[act]\n\ts_mov_b32 %[cnt], 0x100000\n" \
    "1:\n\t" \
    LOAD(208) "s_mov_b64 vcc, exec\n\ts_waitcnt vmcnt(0)\n\t" CHECK(208) GEND "s_sleep 1\n\t" \
    "s_sub_u32 %[cnt], %[cnt], 1\n\ts_cbranch_scc0 1b\n" \
    "9:\n\ts_mov_b64 exec, %[sv]\n\ts_nop 1"
#define GCLOB "vcc", "scc", "memory", "v208", "v209", "v210", "v211", "v212", "v213", "v214", "v215", "v216", "v217", "v218", "v219", "v220", "v221", "v222", "v223", \
    "v224", "v225", "v226", "v227", "v228", "v229", "v230", "v231", "v232", "v233", "v234", "v235", "v236", "v237", "v238", "v239", \
    "v240", "v241", "v242", "v243", "v244", "v245", "v246", "v247", "v248", "v249", "v250", "v251", "v252", "v253", "v254", "v255"
#define GATHER1(SL, rsd, off, tagv, actv, A0, B0) do { u64 sv_, t_; unsigned cnt_; \
    asm volatile(GBODY(G1L, G1C, 2, 1, SL) : [a0] "+v"(A0), [b0] "+v"(B0), [sv] "=&s"(sv_), [t] "=&s"(t_), [cnt] "=&s"(cnt_) \
      : [o] "v"(off), [rs] "s"(rsd), [tag] "s"(tagv), [act] "s"(actv) : GCLOB); } while (0)
#define G1L(R) GL(R, 0, 0)
#define G1C(R) GC(R, 0, a0, b0)
#define G2AL(R) GL(R, 0, 1024) GL(R, 1, 1024)
#define G2BL(R) GL(R, 0, 16) GL(R, 1, 16)
#define G2DL(R) GL(R, 0, 128) GL(R, 1, 128)
#define G2C(R) GC(R, 0, a0, b0) GC(R, 1, a1, b1)
#define G4AL(R) GL(R, 0, 1024) GL(R, 1, 1024) GL(R, 2, 1024) GL(R, 3, 1024)
#define G4BL(R) GL(R, 0, 896) GL(R, 1, 896) GL(R, 2, 896) GL(R, 3, 896)
#define G4C(R) GC(R, 0, a0, b0) GC(R, 1, a1, b1) GC(R, 2, a2, b2) GC(R, 3, a3, b3)
#define GATHER2(LOADM, SL, rsd, off, tagv, actv, D) do { u64 sv_, t_; unsigned cnt_; \
    asm volatile(GBODY(LOADM, G2C, 4, 2, SL) : [a0] "+v"(D[0]), [b0] "+v"(D[1]), [a1] "+v"(D[2]), [b1] "+v"(D[3]), [sv] "=&s"(sv_), [t] "=&s"(t_), [cnt] "=&s"(cnt_) \
      : [o] "v"(off), [rs] "s"(rsd), [tag] "s"(tagv), [act] "s"(actv) : GCLOB); } while (0)
#define GATHER4(LOADM, SL, rsd, off, tagv, actv, D) do { u64 sv_, t_; unsigned cnt_; \
    asm volatile(GBODY(LOADM, G4C, 8, 4, SL) : [a0] "+v"(D[0]), [b0] "+v"(D[1]), [a1] "+v"(D[2]), [b1] "+v"(D[3]), [a2] "+v"(D[4]), [b2] "+v"(D[5]), [a3] "+v"(D[6]), [b3] "+v"(D[7]), \
      [sv] "=&s"(sv_), [t] "=&s"(t_), [cnt] "=&s"(cnt_) \
      : [o] "v"(off), [rs] "s"(rsd), [tag] "s"(tagv), [act] "s"(actv) : GCLOB); } while (0)

#define GL2(R, K) "buffer_load_dwordx4 v[" #R "+4*" #K ":" #R "+4*" #K "+3], %[o2], %[rs], 0 offen sc1\n\t"
#define G3L(R) GL(R, 0, 128) GL(R, 1, 128) GL2(R, 2)
#define G3C(R) GC(R, 0, a0, b0) GC(R, 1, a1, b1) "v_cmp_eq_u32 %[t], %[tag], v[" #R "+9]\n\ts_and_b64 vcc, vcc, %[t]\n\tv_mov_b32 %[a2], v[" #R "+8]\n\t"
#define GATHER3(SL, rsd, off, off2, tagv, actv, D) do { u64 sv_, t_; unsigned cnt_; \
    asm volatile(GBODY(G3L, G3C, 6, 3, SL) : [a0] "+v"(D[0]), [b0] "+v"(D[1]), [a1] "+v"(D[2]), [b1] "+v"(D[3]), [a2] "+v"(D[4]), [sv] "=&s"(sv_), [t] "=&s"(t_), [cnt] "=&s"(cnt_) \
      : [o] "v"(off), [o2] "v"(off2), [rs] "s"(rsd), [tag] "s"(tagv), [act] "s"(actv) : GCLOB); } while (0)
#define GLS(R, K, OFFS, SOFF) "buffer_load_dwordx4 v[" #R "+4*" #K ":" #R "+4*" #K "+3], %[o], %[rs], " SOFF " offen offset:" #OFFS " sc1\n\t"
#define G4CL(R) GLS(R, 0, 0, "0") GLS(R, 1, 2048, "0") GLS(R, 2, 0, "%[so]") GLS(R, 3, 2048, "%[so]")
#define G4DL(R) GLS(R, 0, 0, "0") GLS(R, 1, 1792, "0") GLS(R, 2, 0, "%[so]") GLS(R, 3, 1792, "%[so]")
#define GATHER4S(LOADM, SL, rsd, off, sov, tagv, actv, D) do { u64 sv_, t_; unsigned cnt_; \
    asm volatile(GBODY(LOADM, G4C, 8, 4, SL) : [a0] "+v"(D[0]), [b0] "+v"(D[1]), [a1] "+v"(D[2]), [b1] "+v"(D[3]), [a2] "+v"(D[4]), [b2] "+v"(D[5]), [a3] "+v"(D[6]), [b3] "+v"(D[7]), \
      [sv] "=&s"(sv_), [t] "=&s"(t_), [cnt] "=&s"(cnt_) \
      : [o] "v"(off), [rs] "s"(rsd), [so] "s"(sov), [tag] "s"(tagv), [act] "s"(actv) : GCLOB); } while (0)
#define GATHER4SX(a, b, c, d, e, f, g, h) GATHER4S(a, b, c, d, e, f, g, h)
#define GATHER4X(a, b, c, d, e, f, g) GATHER4(a, b, c, d, e, f, g)
__device__ __forceinline__ v4u ln_row(v4u in) {
  float v[8];
#pragma unroll
  for (int k = 0; k < 4; ++k) { v[2 * k] = lo16(in[k]); v[2 * k + 1] = hi16(in[k]); }
  const float m = xrow_sum(row16_sum(((v[0] + v[1]) + (v[2] + v[3])) + ((v[4] + v[5]) + (v[6] + v[7])))) * (1.f / 512.f);
  float q = 0.f;
#pragma unroll
  for (int j = 0; j < 8; ++j) { const float d = v[j] - m; q = fmaf(d, d, q); }
  const float var = xrow_sum(row16_sum(q)) * (1.f / 512.f);
  const float mr = rbf(m);
  const float den = rbf(__builtin_sqrtf(rbf(rbf(var) + 1e-5f)));
  const float inv = 1.f / den;
  v4u o;
#pragma unroll
  for (int k = 0; k < 4; ++k) {
    const unsigned d = pk(v[2 * k] - mr, v[2 * k + 1] - mr);
    o[k] = pk(lo16(d) * inv, hi16(d) * inv);
  }
  return o;
}

__device__ __forceinline__ void tr8(const v4u* in, v4u (&out)[8]) {
#pragma unroll
  for (int m = 0; m < 4; ++m)
#pragma unroll
    for (int jp = 0; jp < 4; ++jp) {
      out[2 * m][jp] = __builtin_amdgcn_perm(in[jp + 4][m], in[jp][m], 0x05040100u);
      out[2 * m + 1][jp] = __builtin_amdgcn_perm(in[jp + 4][m], in[jp][m], 0x07060302u);
    }
}

template <int SLEEP = 0> __device__ __forceinline__ void lds_wait4(LAS unsigned char* p) {
  for (unsigned spins = 0; spins < (SPIN_CAP << 4); ++spins) {
    const v4u f = *(volatile LAS v4u*)p;
    if (__builtin_amdgcn_readfirstlane(f.x & f.y & f.z & f.w) != 0) break;
    if (SLEEP) __builtin_amdgcn_s_sleep(SLEEP);
  }
  CB();
}

#define SETFLAG(F) do { CB(); *(volatile LAS unsigned*)(L + (F) + w * 4) = 1u; } while (0)

__global__ void __launch_bounds__(512, 2)
fused_kernel(const u16* __restrict__ x, const u16* __restrict__ kc, const u16* __restrict__ vc, const u16* __restrict__ wq,
             const u16* __restrict__ wo, const u16* __restrict__ w1, const u16* __restrict__ b1, const u16* __restrict__ w2,
             const u16* __restrict__ b2, u16* __restrict__ out, unsigned char* __restrict__ ws) {
  __shared__ __attribute__((aligned(16))) unsigned char smem[LDS_BYTES + 16];
  u64 tstart; asm volatile("s_memrealtime %0" : "=s"(tstart));
  LAS unsigned char* L = (LAS unsigned char*)smem;
  STAMP(0);
  const int tid = threadIdx.x, lane = tid & 63, wave = __builtin_amdgcn_readfirstlane(tid >> 6);
  const int wg = blockIdx.x, kvh = wg & 7, r = wg >> 3, b = r >> 3, s = r & 7;
  const int l16 = lane & 15, c = lane >> 4;
  const int ks = wg >> 2, ns = wg & 3;
  if (tid < 44) *(LAS unsigned*)(L + L_FLAG + tid * 4) = 0u;
  unsigned pf = 0;
  if (wave == 3) {
    u64 pc; unsigned len;
    asm volatile("s_getpc_b64 %0\n.Lpc_here:\n\ts_mov_b32 %1, .Lk_end-.Lpc_here" : "=s"(pc), "=s"(len));
    pf = ld32<0>(mkrs((const void*)pc, len), r * 512 + lane * 8, 0);
  }
  asm volatile("" :: "s"(x), "s"(kc), "s"(vc), "s"(wq), "s"(wo), "s"(w1), "s"(b1), "s"(w2), "s"(b2), "s"(out), "s"(ws));
  BAR();
  const rsrc_t rws = mkrs(ws, WS_BYTES);
  const unsigned ep = *(const __attribute__((address_space(4))) unsigned*)(ws + EP_OFF);
  unsigned tag;
#define MAKE_TAG() asm volatile("s_add_u32 %0, %1, 1" : "=s"(tag) : "s"(ep) : "scc")
  const int w = wave & 3;
  const size_t kvbase = ((size_t)(b * 8 + kvh) * 8192 + 6144) * 128;
  int tso[4];
#pragma unroll
  for (int t = 0; t < 4; ++t) tso[t] = (32 * t + ((4 * s + w + ROT * t) & 31)) * 4096;
  const int dup = l16 & 3;
  const int u1 = kvh * 14 + r;
  const int kq = r & 3, kq1 = u1 & 3;

  v4u xr, wv[4], uv[4], K[16];
  const rsrc_t rk = mkrs(kc + kvbase, 2048 * 256);
  const int koff = (l16 >> 2) * 256 + c * 64 + dup * 16;
  if (wave >= 4) {
    __builtin_amdgcn_s_setprio(VPRIO);
    const rsrc_t rwq = mkrs(wq + kvh * 512 + (r >> 2) * 64, (EXP & 2) ? 0 : 512 * 8192);
    const rsrc_t rw1 = mkrs(w1 + (u1 >> 2) * 64, (r < 14 && !(EXP & 1)) ? 512 * 3584 : 0);
    xr = ld128<0>(mkrs(x, 4096), w * 1024 + lane * 16, 0);
    SB();
#pragma unroll
    for (int n = 0; n < 4; ++n) wv[n] = ld128<AUX_NT>(rwq, (lane >> 3) * 8192 + (lane & 7) * 16, (kq * 128 + w * 32 + n * 8) * 8192);
    SB();
#pragma unroll
    for (int n = 0; n < 4; ++n) uv[n] = ld128<AUX_NT>(rw1, (lane >> 3) * 3584 + (lane & 7) * 16, (kq1 * 128 + w * 32 + n * 8) * 3584);
    SB();
  } else {
    __builtin_amdgcn_s_sleep(KDELAY);
#pragma unroll
    for (int n = 0; n < WK; ++n) K[n] = ld128<AUX_NT>(rk, koff + (n & 3) * 1024, tso[n >> 2]);
    SB();
  }
  int wave2 = wave;
  asm volatile("" : "+s"(wave2));

  if (wave2 < 4) {
    const int qoff = QP_OFF + ((kvh * 4 + b) * 4 + w) * 4096 + lane * 16;
    v4u Q[4];
#pragma unroll
    for (int n = WK; n < 16; ++n) {
      asm volatile("s_waitcnt vmcnt(%0)" :: "n"(WK - 1) : "memory");
      K[n] = ld128<AUX_NT>(rk, koff + (n & 3) * 1024, tso[n >> 2]);
      SB();
      if (n == QAT) {
#pragma unroll
        for (int k = 0; k < 4; ++k) Q[k] = ld128<AUX_SC1>(rws, qoff + k * 1024, 0);
        SB();
      }
    }
    MAKE_TAG();
    SETFLAG(F_K);
    __builtin_amdgcn_s_setprio(KPRIO);
    asm volatile("" :: "v"(pf));
    STAMP(1);
    {
      float qd[8];
      unsigned bad = 0;
#pragma unroll
      for (int k = 0; k < 4; ++k) { qd[2 * k] = __uint_as_float(Q[k].x); qd[2 * k + 1] = __uint_as_float(Q[k].z); bad |= (Q[k].y ^ tag) | (Q[k].w ^ tag); }
      const u64 miss = __builtin_amdgcn_ballot_w64(bad != 0);
      if (miss) GATHER4X(G4AL, QSL, rws, qoff, tag, miss, qd);
      *(LAS unsigned*)(L + L_Q + w * 256 + lane * 4) = pk((qd[0] + qd[2]) + (qd[4] + qd[6]), (qd[1] + qd[3]) + (qd[5] + qd[7]));
      SETFLAG(F_Q);
    }
    v4u Z[4];
    unsigned bias = 0;
    const int zoff = wg < 224 ? ZP_OFF + ks * 4096 + lane * 16 : OOB;
    if (wave2 == 3) {
#pragma unroll
      for (int k = 0; k < 4; ++k) Z[k] = ld128<AUX_SC1>(rws, zoff + k * 1024, 0);
      bias = ld32<0>(mkrs(b1, 3584), (ks * 32 + l16 * 2) * 2, 0);
      SB();
    }
    STAMP(2);
    lds_wait4(L + F_Q);
    STAMP(13);
    const v4u qf = *(LAS v4u*)(L + L_Q + dup * 256 + c * 64 + (l16 >> 2) * 16);
    float sc[4];
#pragma unroll
    for (int mm = 0; mm < 4; ++mm) {
      float t[4];
#pragma unroll
      for (int be = 0; be < 4; ++be) {
        const f32x4 d = mfma(K[4 * mm + be], qf, f32x4{0.f, 0.f, 0.f, 0.f});
        t[be] = ((d[be] + dpp<0x12C>(d[(be + 1) & 3])) + dpp<0x128>(d[(be + 2) & 3])) + dpp<0x124>(d[(be + 3) & 3]);
      }
      const float t01 = (l16 & 4) ? t[1] : t[0], t23 = (l16 & 4) ? t[3] : t[2];
      sc[mm] = (l16 & 8) ? t23 : t01;
    }
    {
      const unsigned d0 = pk(sc[0], sc[1]), d1 = pk(sc[2], sc[3]);
      const unsigned e0 = pk(lo16(d0) * 0.08838834764831845f, hi16(d0) * 0.08838834764831845f);
      const unsigned e1 = pk(lo16(d1) * 0.08838834764831845f, hi16(d1) * 0.08838834764831845f);
      sc[0] = lo16(e0); sc[1] = hi16(e0); sc[2] = lo16(e1); sc[3] = hi16(e1);
    }
    float m = fmaxf(fmaxf(sc[0], sc[1]), fmaxf(sc[2], sc[3]));
    m = fmaxf(m, dpp<0x124>(m)); m = fmaxf(m, dpp<0x128>(m));
    m = xrow_max(m);
    const float p0 = ex2((sc[0] - m) * LOG2E), p1 = ex2((sc[1] - m) * LOG2E), p2 = ex2((sc[2] - m) * LOG2E), p3 = ex2((sc[3] - m) * LOG2E);
    *(LAS unsigned*)(L + L_P + w * 512 + dup * 64 + c * 16 + (l16 >> 2) * 4) = pk(p0, p1);
    *(LAS unsigned*)(L + L_P + w * 512 + 256 + dup * 64 + c * 16 + (l16 >> 2) * 4) = pk(p2, p3);
    float l = (p0 + p1) + (p2 + p3);
    l += dpp<0x124>(l); l += dpp<0x128>(l);
    l = xrow_sum(l);
    if (lane < 4) *(LAS f32x2*)(L + L_ST + lane * 32 + w * 8) = f32x2{m, l};
    SETFLAG(F_P);
    STAMP(3);
    if (wave2 == 3) {
      float zd[8];
      unsigned bad = 0;
#pragma unroll
      for (int k = 0; k < 4; ++k) { zd[2 * k] = __uint_as_float(Z[k].x); zd[2 * k + 1] = __uint_as_float(Z[k].z); bad |= (Z[k].y ^ tag) | (Z[k].w ^ tag); }
      const u64 miss = __builtin_amdgcn_ballot_w64(bad != 0);
      if (wg < 224 && miss) GATHER4(G4AL, 8, rws, zoff, tag, miss, zd);
      const unsigned zp = pk((zd[0] + zd[2]) + (zd[4] + zd[6]), (zd[1] + zd[3]) + (zd[5] + zd[7]));
      const unsigned zb = pk(lo16(zp) + lo16(bias), hi16(zp) + hi16(bias));
      float z[2] = {lo16(zb), hi16(zb)};
#pragma unroll
      for (int i = 0; i < 2; ++i) {
        const float zz = z[i];
        const float u = 0.7978845608028654f * (zz + 0.044715f * zz * zz * zz);
        const float th = 1.f - 2.f * rcp(ex2(u * (2.f * LOG2E)) + 1.f);
        z[i] = 0.5f * zz * (1.f + th);
      }
      *(LAS unsigned*)(L + L_H + lane * 4) = pk(z[0], z[1]);
      CB();
      if (lane < 4) *(volatile LAS unsigned*)(L + F_H + lane * 4) = 1u;
    }
    STAMP(4);
    const int oidx = (wg >> 6) * 512 + (wg & 7) * 64 + ((wg >> 5) & 1) * 32 + ((wg >> 3) & 3) * 4 + ((lane >> 1) & 1) * 16 + (lane & 1) * 2;
    const unsigned xw = ld32<0>(mkrs(x, 4096), oidx * 2, 0);
    const unsigned bw = ld32<0>(mkrs(b2, 1024), (oidx & 511) * 2, 0);
    const int sp = lane & 7, k4 = lane >> 3;
    const int osw = L_OS + w * OS_STRIDE + k4 * 8;
    const int p1g = P1_OFF + (ks * 32 + ((w * 8 + sp + SKEW * ks) & 31)) * 256 + k4 * 16;
    const int l1g = L1_OFF + (((ks >> 1) * 4 + w) * 8 + sp) * 128;
    asm volatile("" :: "v"(osw), "v"(l1g), "v"(p1g), "v"(xw), "v"(bw));
    lds_wait4<2>(L + F_O);
    __builtin_amdgcn_s_sleep(PSL);
    STAMP(12);
    unsigned d[5] = {0u, 0u, 0u, 0u, 0u};
    GATHER3(4, rws, p1g, l1g, tag, ~0ull, d);
    STAMP(6);
    {
      const float lse = __uint_as_float(d[4]);
      const float e = ex2(lse - oct_max(lse));
      const float inv = rcp(oct_sum(e));
      float a[8];
#pragma unroll
      for (int k = 0; k < 4; ++k) { a[2 * k] = oct_sum(e * lo16(d[k])) * inv; a[2 * k + 1] = oct_sum(e * hi16(d[k])) * inv; }
      if (sp == 0) {
        *(LAS v2u*)(L + osw) = v2u{pk(a[0], a[1]), pk(a[2], a[3])};
        *(LAS v2u*)(L + osw + 64) = v2u{pk(a[4], a[5]), pk(a[6], a[7])};
      }
      SETFLAG(F_OS);
    }
    const int foff = ((wg >> 7) * 64 + (wg & 63)) * 8192 + ((wg >> 6) & 1) * 64 + (lane >> 2) * 128 + (lane & 3) * 16;
    if (wave2 == 1) {
      float dg[8] = {0.f, 0.f, 0.f, 0.f, 0.f, 0.f, 0.f, 0.f};
      GATHER4SX(G4DL, GPS, rws, G2_OFF + foff, 3584, tag, (1ull << 56) - 1ull, dg);
      float g0 = (dg[0] + dg[2]) + (dg[4] + dg[6]);
      float g1 = (dg[1] + dg[3]) + (dg[5] + dg[7]);
      g0 += dpp<0x124>(g0); g1 += dpp<0x124>(g1);
      g0 += dpp<0x128>(g0); g1 += dpp<0x128>(g1);
      g0 = xrow_sum(g0); g1 = xrow_sum(g1);
      const unsigned gp = pk(g0, g1);
      const unsigned gb = pk(lo16(gp) + lo16(bw), hi16(gp) + hi16(bw));
      const float z0 = lo16(gb), z1 = hi16(gb);
      const unsigned sg = pk(z0 * rcp(1.f + ex2(-z0 * LOG2E)), z1 * rcp(1.f + ex2(-z1 * LOG2E)));
      if (lane < 4) *(volatile LAS v2u*)(L + F_G + lane * 8) = v2u{sg, 1u};
      STAMP(8);
    } else if (wave2 == 0) {
      __builtin_amdgcn_s_sleep(FSL);
      float ds[8] = {0.f, 0.f, 0.f, 0.f, 0.f, 0.f, 0.f, 0.f};
      GATHER4SX(G4CL, FPS, rws, P2_OFF + foff, 4096, tag, ~0ull, ds);
      STAMP(9);
      st128(rws, (wg == 0 && lane < 4) ? EP_OFF + lane * 16 : OOB, v4u{tag, tag, tag, tag});
      v2u sgt = *(volatile LAS v2u*)(L + F_G + (lane & 3) * 8);
      float s0 = (ds[0] + ds[2]) + (ds[4] + ds[6]);
      float s1 = (ds[1] + ds[3]) + (ds[5] + ds[7]);
      s0 += dpp<0x124>(s0); s1 += dpp<0x124>(s1);
      s0 += dpp<0x128>(s0); s1 += dpp<0x128>(s1);
      s0 = xrow_sum(s0); s1 = xrow_sum(s1);
      const unsigned sq = pk(s0, s1);
      for (unsigned spins = 0; !__all(sgt.y != 0u) && spins < (SPIN_CAP << 4); ++spins) sgt = *(volatile LAS v2u*)(L + F_G + (lane & 3) * 8);
      const unsigned pr = pk(lo16(sq) * lo16(sgt.x), hi16(sq) * hi16(sgt.x));
      const unsigned res = pk(lo16(xw) + lo16(pr), hi16(xw) + hi16(pr));
      if (lane < 4) *(GAS unsigned*)(out + oidx) = res;
      STAMP(10);
      {
        const u64 tx = PADN >= 0 ? *(volatile LAS u64*)(L + LDS_BYTES) : tstart;
        const u64 target = (wg & 7) == 0 ? tx + 4000 : tx;
        for (;;) { u64 t_; asm volatile("s_memrealtime %0\n\ts_waitcnt lgkmcnt(0)" : "=s"(t_) :: "memory"); if (t_ >= target) break; __builtin_amdgcn_s_sleep(1); }
      }
    }
  } else {
    const v4u xn = ln_row(xr);
    *(LAS v4u*)(L + L_XN + w * XS_STRIDE + lane * 16) = xn;
    MAKE_TAG();
    STAMP(1);
    const int sw = (w * 32 + ((lane >> 5) & 1) * 8 + ((lane >> 3) & 3)) * WT_RS + (lane & 7) * 16;
#pragma unroll
    for (int n = 0; n < 4; ++n) *(LAS v4u*)(L + L_WQ + sw + ((n >> 1) * 16 + (n & 1) * 4) * WT_RS) = wv[n];
    SETFLAG(F_A);
    const int ta = ((c >> 1) * 16 + (c & 1) * 4 + (l16 >> 2)) * WT_RS + w * 32 + (l16 & 3) * 8;
    const int xb = (l16 >> 2) * XS_STRIDE + c * 16;
    lds_wait4(L + F_A);
    {
      f32x4 acc = {0.f, 0.f, 0.f, 0.f};
#pragma unroll
      for (int kk = 0; kk < 4; ++kk) {
        const v2u a0 = trread(L + L_WQ + ta + kk * 32 * WT_RS), a1 = trread(L + L_WQ + ta + (kk * 32 + 8) * WT_RS);
        acc = mfma(*(LAS v4u*)(L + L_XN + xb + kq * 256 + kk * 64), v4u{a0.x, a0.y, a1.x, a1.y}, acc);
      }
      st64(rws, QP_OFF + ((((kvh * 4 + c) * 4 + (r >> 3)) * 4 + kq) * 128 + ((r >> 2) & 1) * 64 + w * 16 + l16) * 8, v2u{__float_as_uint(acc[0]), tag});
    }
    SB();
    STAMP(2);
    const v4u yn = ln_row(xn);
    *(LAS v4u*)(L + L_YN + w * XS_STRIDE + lane * 16) = yn;
#pragma unroll
    for (int n = 0; n < 4; ++n) *(LAS v4u*)(L + L_W1 + sw + ((n >> 1) * 16 + (n & 1) * 4) * WT_RS) = uv[n];
    SETFLAG(F_B);
    lds_wait4(L + F_B);
    {
      f32x4 acc = {0.f, 0.f, 0.f, 0.f};
#pragma unroll
      for (int kk = 0; kk < 4; ++kk) {
        const v2u a0 = trread(L + L_W1 + ta + kk * 32 * WT_RS), a1 = trread(L + L_W1 + ta + (kk * 32 + 8) * WT_RS);
        acc = mfma(*(LAS v4u*)(L + L_YN + xb + kq1 * 256 + kk * 64), v4u{a0.x, a0.y, a1.x, a1.y}, acc);
      }
      const int off = ZP_OFF + (((((u1 >> 2) * 2 + (w >> 1)) * 4 + kq1) * 4 + c) * 32 + (w & 1) * 16 + l16) * 8;
      st64(rws, r < 14 ? off : OOB, v2u{__float_as_uint(acc[0]), tag});
    }
    __builtin_amdgcn_s_setprio(0);
    STAMP(3);
    for (unsigned spins = 0; spins < (SPIN_CAP << 4); ++spins) {
      if (*(volatile LAS unsigned*)(L + F_K + w * 4) != 0u) break;
      __builtin_amdgcn_s_sleep(1);
    }
    const rsrc_t rv = mkrs(vc + kvbase, 2048 * 256);
    const int voff = lane * 16;
    const rsrc_t rwo = mkrs(wo + (size_t)ks * 64 * 512 + ns * 128, 64 * 1024);
    const rsrc_t rw2 = mkrs(w2 + (size_t)ks * 32 * 512 + ns * 128, wg < 224 ? 32 * 1024 : 0);
    v4u V[16], WO[6];
#pragma unroll
    for (int n = 0; n < 16; ++n) {
      if (n >= WV) asm volatile("s_waitcnt vmcnt(%0)" :: "n"(WV - 1) : "memory");
      V[n] = ld128<AUX_NT>(rv, voff + (n & 3) * 1024, tso[n >> 2]);
      SB();
      if (n < WV - 1 && VRAMP) { __builtin_amdgcn_s_sleep(VRAMP); SB(); }
    }
    STAMP(4);
#define WO_LOAD(n) do { asm volatile("s_waitcnt vmcnt(%0)" :: "n"(WV - 1) : "memory"); \
      WO[n] = (n) < 4 ? ld128<AUX_NT>(rwo, (16 * w + c) * 1024 + l16 * 16, (n) * 4096) : ld128<AUX_NT>(rw2, (8 * w + c) * 1024 + l16 * 16, ((n) - 4) * 4096); SB(); } while (0)
    WO_LOAD(0); WO_LOAD(1);
    lds_wait4<1>(L + F_P);
    v4u P[2];
    P[0] = *(LAS v4u*)(L + L_P + w * 512 + (l16 >> 2) * 64 + c * 16);
    P[1] = *(LAS v4u*)(L + L_P + w * 512 + 256 + (l16 >> 2) * 64 + c * 16);
    float wgt[4];
    unsigned lse2;
    {
      const f32x4 s0 = *(LAS f32x4*)(L + L_ST + w * 32), s1 = *(LAS f32x4*)(L + L_ST + w * 32 + 16);
      const float M = fmaxf(fmaxf(s0[0], s0[2]), fmaxf(s1[0], s1[2]));
      wgt[0] = ex2((s0[0] - M) * LOG2E);
      wgt[1] = ex2((s0[2] - M) * LOG2E);
      wgt[2] = ex2((s1[0] - M) * LOG2E);
      wgt[3] = ex2((s1[2] - M) * LOG2E);
      const float Ls = wgt[0] * s0[1] + wgt[1] * s0[3] + wgt[2] * s1[1] + wgt[3] * s1[3];
      const float inv = rcp(Ls);
#pragma unroll
      for (int k = 0; k < 4; ++k) wgt[k] *= inv;
      lse2 = __float_as_uint(M * LOG2E + __builtin_amdgcn_logf(Ls));
    }
    const int ksp = (kvh * 4 + w) * 2 + (c & 1);
    const int p1off = lane < 32 ? P1_OFF + (ksp * 32 + ((r + SKEW * ksp) & 31)) * 256 + l16 * 16
                    : lane < 40 ? L1_OFF + (((kvh * 4 + w) * 4 + b) * 8 + s) * 128 + l16 * 16 : OOB;
    const int ppw = L_PART + w * 2048 + c * 512 + l16 * 32, ppr = L_PART + w * 512 + (lane & 31) * 16;
    f32x4 o[8];
    {
      v4u vt[8];
      tr8(&V[0], vt);
#pragma unroll
      for (int e = 0; e < 8; ++e) o[e] = mfma(P[0], vt[e], f32x4{0.f, 0.f, 0.f, 0.f});
    }
    asm volatile("" :: "v"(p1off), "v"(ppw), "v"(ppr), "v"(wgt[0]), "v"(wgt[1]), "v"(wgt[2]), "v"(wgt[3]), "v"(lse2));
    SB();
    WO_LOAD(2); WO_LOAD(3); WO_LOAD(4); WO_LOAD(5);
    {
      v4u vt[8];
      tr8(&V[8], vt);
#pragma unroll
      for (int e = 0; e < 8; ++e) o[e] = mfma(P[1], vt[e], o[e]);
    }
    ((LAS f32x4*)(L + ppw))[0] = f32x4{o[0][0], o[1][0], o[2][0], o[3][0]};
    ((LAS f32x4*)(L + ppw))[1] = f32x4{o[4][0], o[5][0], o[6][0], o[7][0]};
    SETFLAG(F_O);
    STAMP(11);
    lds_wait4(L + F_O);
    {
      const LAS f32x4* rp = (const LAS f32x4*)(L + ppr);
      const f32x4 a = (wgt[0] * rp[0] + wgt[1] * rp[128]) + (wgt[2] * rp[256] + wgt[3] * rp[384]);
      st128(rws, p1off, v4u{lane < 32 ? pk(a[0], a[1]) : lse2, tag, pk(a[2], a[3]), tag});
    }
    STAMP(5);
#pragma unroll
    for (int n = 0; n < 4; ++n) *(LAS v4u*)(L + L_WO + swap23(16 * w + 4 * n + c) * WO_RS + l16 * 16) = WO[n];
#pragma unroll
    for (int n = 0; n < 2; ++n) *(LAS v4u*)(L + L_WO + (64 + swap23(8 * w + 4 * n + c)) * WO_RS + l16 * 16) = WO[4 + n];
    SETFLAG(F_WO);
    STAMP(14);
    const int fpair = (ns * 2 + (w >> 1)) + 8 * ((l16 >> 3) * 8 + (w & 1) * 4 + c);
    const int sk = ks + SKEW * (fpair >> 3);
    const int p2off = P2_OFF + (fpair * 64 + (sk & 63)) * 128 + (l16 & 7) * 16;
    const int g2off = G2_OFF + (fpair * 64 + (sk >= 56 ? sk - 56 : sk)) * 128 + (l16 & 7) * 16;
    const int osr = L_OS + (l16 >> 2) * OS_STRIDE + c * 16;
    lds_wait4(L + F_WO);
    v4u af[2][2];
    {
      LAS unsigned char* tw = L + L_WO + ((c >> 1) * 16 + (c & 1) * 4 + (l16 >> 2)) * WO_RS + w * 64 + (l16 & 3) * 8;
#pragma unroll
      for (int ct = 0; ct < 2; ++ct)
#pragma unroll
        for (int kk = 0; kk < 2; ++kk) {
          const v2u a0 = trread(tw + ct * 32 + kk * 32 * WO_RS), a1 = trread(tw + ct * 32 + kk * 32 * WO_RS + 8 * WO_RS);
          af[ct][kk] = v4u{a0.x, a0.y, a1.x, a1.y};
        }
      lds_wait4<1>(L + F_H);
      const v4u hb = *(LAS v4u*)(L + L_H + (l16 >> 2) * 64 + c * 16);
      f32x4 g[2];
#pragma unroll
      for (int ct = 0; ct < 2; ++ct) {
        const v2u a0 = trread(tw + ct * 32 + 64 * WO_RS), a1 = trread(tw + ct * 32 + 72 * WO_RS);
        g[ct] = mfma(v4u{a0.x, a0.y, a1.x, a1.y}, hb, f32x4{0.f, 0.f, 0.f, 0.f});
      }
      const f32x4 acc = (dup & 2) ? g[1] : g[0];
      const float v0 = (dup & 1) ? acc[2] : acc[0], v1 = (dup & 1) ? acc[3] : acc[1];
      st128(rws, wg < 224 ? g2off : OOB, v4u{__float_as_uint(v0), tag, __float_as_uint(v1), tag});
    }
    asm volatile("" :: "v"(p2off), "v"(osr), "v"(af[0][0]), "v"(af[0][1]), "v"(af[1][0]), "v"(af[1][1]));
    STAMP(6);
    lds_wait4(L + F_OS);
    {
      const v4u b0 = *(LAS v4u*)(L + osr), b1f = *(LAS v4u*)(L + osr + 64);
      f32x4 acc0 = mfma(af[0][0], b0, f32x4{0.f, 0.f, 0.f, 0.f});
      f32x4 acc1 = mfma(af[1][0], b0, f32x4{0.f, 0.f, 0.f, 0.f});
      acc0 = mfma(af[0][1], b1f, acc0);
      acc1 = mfma(af[1][1], b1f, acc1);
      const f32x4 acc = (dup & 2) ? acc1 : acc0;
      const float v0 = (dup & 1) ? acc[2] : acc[0], v1 = (dup & 1) ? acc[3] : acc[1];
      st128(rws, p2off, v4u{__float_as_uint(v0), tag, __float_as_uint(v1), tag});
    }
    STAMP(7);
  }
  asm volatile(".Lk_end:");
}

extern "C" void kernel_launch(void* const* d_in, const int* in_sizes, int n_in,
                              void* d_out, int out_size, void* d_ws, size_t ws_size,
                              hipStream_t stream) {
  fused_kernel<<<256, 512, 0, stream>>>((const u16*)d_in[0], (const u16*)d_in[1], (const u16*)d_in[2], (const u16*)d_in[3],
                                        (const u16*)d_in[4], (const u16*)d_in[6], (const u16*)d_in[7], (const u16*)d_in[8],
                                        (const u16*)d_in[9], (u16*)d_out, (unsigned char*)d_ws);
}
```

```cpp
#include <hip/hip_runtime.h>
#include <stdint.h>

#define GAS __attribute__((address_space(1)))
#define LAS __attribute__((address_space(3)))
#define CAS __attribute__((address_space(4)))
typedef unsigned short u16;
typedef unsigned long long u64;
typedef unsigned v4u __attribute__((ext_vector_type(4)));
typedef unsigned v2u __attribute__((ext_vector_type(2)));
typedef short v4s __attribute__((ext_vector_type(4)));
typedef float f32x4 __attribute__((ext_vector_type(4)));
typedef float f32x2 __attribute__((ext_vector_type(2)));
typedef __bf16 v8bf __attribute__((ext_vector_type(8)));
typedef __bf16 v2bf __attribute__((ext_vector_type(2)));
typedef __amdgpu_buffer_rsrc_t rsrc_t;

#define AUXNT 2
constexpr int AUX_NT = AUXNT, AUX_SC1 = 16;
constexpr unsigned SPIN_CAP = 1u << 22;
constexpr int OOB = 0x7fff0000;
constexpr float LOG2E = 1.4426950408889634f;

#define EXP 0
#define PFW 4
#define GSL 12
#define RN 9
#define XT 610
#define WK 6
#define WV 7
#define KDELAY 4
#define QDELAY 12
#define ROT 9
#define SKEW 2
#define XMAP 3
#define XQ 1
#define WOAT 1
#define PSL 18
#define FSL 0

constexpr int EP_OFF = 0;
constexpr int QP_OFF = 4096;
constexpr int ZP_OFF = QP_OFF + 524288;
constexpr int L1_OFF = ZP_OFF + 458752;
constexpr int P1_OFF = L1_OFF + 131072;
constexpr int P2_OFF = P1_OFF + 524288;
constexpr int G2_OFF = P2_OFF + 1048576;
constexpr int WS_BYTES = G2_OFF + 1048576;
constexpr int ST_OFF = 8 << 20;

constexpr int XS_STRIDE = 1024 + 64;
constexpr int WT_RS = 160;
constexpr int L_WQ = 0;
constexpr int L_W1 = L_WQ + 128 * WT_RS;
constexpr int L_XN = L_W1 + 64 * WT_RS;
constexpr int L_YN = L_XN + 4 * XS_STRIDE;
constexpr int L_Q = L_YN + 4 * XS_STRIDE;
constexpr int L_ST = L_Q + 1024;
constexpr int L_FLAG = L_ST + 128;
constexpr int F_A = L_FLAG, F_B = L_FLAG + 16, F_Q = L_FLAG + 32, F_K = L_FLAG + 48, F_P = L_FLAG + 64, F_O = L_FLAG + 80, F_WO = L_FLAG + 96,
              F_OS = L_FLAG + 112, F_G = L_FLAG + 128, F_H = L_FLAG + 160, F_N = L_FLAG + 176, F_T = L_FLAG + 144;
constexpr int L_P = L_FLAG + 192;
constexpr int L_PART = L_P + 2048;
constexpr int WO_RS = 288;
constexpr int L_WO = L_PART + 8192;
constexpr int OS_STRIDE = 128 + 16;
constexpr int L_OS = L_WO + 96 * WO_RS;
constexpr int L_H = L_OS + 32 * OS_STRIDE;
constexpr int LDS_BYTES = L_H + 256;

__device__ __forceinline__ unsigned pk(float lo, float hi) { f32x2 v = {lo, hi}; return __builtin_bit_cast(unsigned, __builtin_convertvector(v, v2bf)); }
__device__ __forceinline__ float lo16(unsigned u) { return __builtin_bit_cast(float, u << 16); }
__device__ __forceinline__ float hi16(unsigned u) { return __builtin_bit_cast(float, u & 0xffff0000u); }
__device__ __forceinline__ float rbf(float x) { return lo16(pk(x, x)); }
__device__ __forceinline__ float rcp(float x) { return __builtin_amdgcn_rcpf(x); }
__device__ __forceinline__ float ex2(float x) { return __builtin_amdgcn_exp2f(x); }
template <int CTRL> __device__ __forceinline__ float dpp(float x) {
  return __builtin_bit_cast(float, __builtin_amdgcn_mov_dpp(__builtin_bit_cast(int, x), CTRL, 0xf, 0xf, true));
}
__device__ __forceinline__ float row16_sum(float x) { x += dpp<0x128>(x); x += dpp<0x124>(x); x += dpp<0x122>(x); x += dpp<0x121>(x); return x; }
__device__ __forceinline__ float oct_sum(float x) { x += dpp<0xB1>(x); x += dpp<0x4E>(x); x += dpp<0x141>(x); return x; }
__device__ __forceinline__ float oct_max(float x) { x = fmaxf(x, dpp<0xB1>(x)); x = fmaxf(x, dpp<0x4E>(x)); x = fmaxf(x, dpp<0x141>(x)); return x; }
__device__ __forceinline__ float xrow_max(float x) {
  auto s = __builtin_amdgcn_permlane16_swap(__float_as_uint(x), __float_as_uint(x), false, false);
  x = fmaxf(__uint_as_float(s[0]), __uint_as_float(s[1]));
  auto t = __builtin_amdgcn_permlane32_swap(__float_as_uint(x), __float_as_uint(x), false, false);
  return fmaxf(__uint_as_float(t[0]), __uint_as_float(t[1]));
}
__device__ __forceinline__ float xrow_sum(float x) {
  auto s = __builtin_amdgcn_permlane16_swap(__float_as_uint(x), __float_as_uint(x), false, false);
  x = __uint_as_float(s[0]) + __uint_as_float(s[1]);
  auto t = __builtin_amdgcn_permlane32_swap(__float_as_uint(x), __float_as_uint(x), false, false);
  return __uint_as_float(t[0]) + __uint_as_float(t[1]);
}
__device__ __forceinline__ f32x4 mfma(v4u a, v4u b, f32x4 c) {
  return __builtin_amdgcn_mfma_f32_16x16x32_bf16(__builtin_bit_cast(v8bf, a), __builtin_bit_cast(v8bf, b), c, 0, 0, 0);
}
__device__ __forceinline__ rsrc_t mkrs(const void* p, int bytes) { return __builtin_amdgcn_make_buffer_rsrc((void*)p, 0, bytes, 0x00020000); }
template <int AUX> __device__ __forceinline__ v4u ld128(rsrc_t r, int voff, int soff) { return __builtin_amdgcn_raw_buffer_load_b128(r, voff, soff, AUX); }
template <int AUX> __device__ __forceinline__ unsigned ld32(rsrc_t r, int voff, int soff) { return __builtin_amdgcn_raw_buffer_load_b32(r, voff, soff, AUX); }
__device__ __forceinline__ void st128(rsrc_t r, int voff, v4u v) { __builtin_amdgcn_raw_buffer_store_b128(v, r, voff, 0, AUX_SC1); }
__device__ __forceinline__ void st64(rsrc_t r, int voff, v2u v) { __builtin_amdgcn_raw_buffer_store_b64(v, r, voff, 0, AUX_SC1); }
__device__ __forceinline__ v2u trread(LAS unsigned char* p) { return __builtin_bit_cast(v2u, __builtin_amdgcn_ds_read_tr16_b64_v4i16((LAS v4s*)p)); }
#define SB() __builtin_amdgcn_sched_barrier(0)
#define CB() asm volatile("" ::: "memory")
#define WAITVM(n) asm volatile("s_waitcnt vmcnt(%0)" :: "n"(n) : "memory")
__device__ __forceinline__ int swap23(int k) { return (k & ~12) | ((k & 4) << 1) | ((k & 8) >> 1); }

#define STAMP(n) do {} while (0)
#define DUMP_STAMPS() do {} while (0)

#define GBODY(LOADS, CHECKS) \
    "s_mov_b64 %[sv], exec\n\ts_mov_b64 exec, %[act]\n\ts_mov_b32 %[cnt], 0x100000\n" \
    "1:\n\t" LOADS "s_waitcnt vmcnt(0)\n\tv_xor_b32 %[t], %[tag], v153\n\t" CHECKS \
    "v_cmp_ne_u32 vcc, 0, %[t]\n\ts_and_b64 exec, exec, vcc\n\ts_cbranch_scc0 9f\n\ts_sleep 1\n\ts_sub_u32 %[cnt], %[cnt], 1\n\ts_cbranch_scc0 1b\n" \
    "9:\n\ts_mov_b64 exec, %[sv]"
#define GL(R, O, OFFS, SOFF) "buffer_load_dwordx4 " R ", %[" #O "], %[rs], " SOFF " offen offset:" #OFFS " sc1\n\t"
#define GC(R) "v_bitop3_b32 %[t], " R ", %[tag], %[t] bitop3:0xbe\n\t"
#define GATHER4(S1, rsd, off, sov, tagv, actv, D) do { u64 sv_; unsigned cnt_, t_; \
    asm volatile(GBODY(GL("v[152:155]", o, 0, "0") GL("v[156:159]", o, S1, "0") GL("v[160:163]", o, 0, "%[so]") GL("v[164:167]", o, S1, "%[so]"), \
                       GC("v155") GC("v157") GC("v159") GC("v161") GC("v163") GC("v165") GC("v167")) \
      : "+{v[152:155]}"(D[0]), "+{v[156:159]}"(D[1]), "+{v[160:163]}"(D[2]), "+{v[164:167]}"(D[3]), [sv] "=&s"(sv_), [t] "=&v"(t_), [cnt] "=&s"(cnt_) \
      : [o] "v"(off), [rs] "s"(rsd), [so] "s"(sov), [tag] "s"(tagv), [act] "s"(actv) : "vcc", "scc", "memory"); } while (0)
#define GATHER3(rsd, off, off2, tagv, actv, D) do { u64 sv_; unsigned cnt_, t_; \
    asm volatile(GBODY(GL("v[152:155]", o, 0, "0") GL("v[156:159]", o, 128, "0") GL("v[160:163]", o2, 0, "0"), GC("v155") GC("v157") GC("v159") GC("v161")) \
      : "={v[152:155]}"(D[0]), "={v[156:159]}"(D[1]), "={v[160:163]}"(D[2]), [sv] "=&s"(sv_), [t] "=&v"(t_), [cnt] "=&s"(cnt_) \
      : [o] "v"(off), [o2] "v"(off2), [rs] "s"(rsd), [tag] "s"(tagv), [act] "s"(actv) : "vcc", "scc", "memory"); } while (0)
__device__ __forceinline__ float sumx(const v4u* d) { return (__uint_as_float(d[0].x) + __uint_as_float(d[1].x)) + (__uint_as_float(d[2].x) + __uint_as_float(d[3].x)); }
__device__ __forceinline__ float sumz(const v4u* d) { return (__uint_as_float(d[0].z) + __uint_as_float(d[1].z)) + (__uint_as_float(d[2].z) + __uint_as_float(d[3].z)); }
__device__ __forceinline__ unsigned tagdiff(const v4u* d, unsigned tag) {
  return ((d[0].y ^ tag) | (d[0].w ^ tag) | (d[1].y ^ tag) | (d[1].w ^ tag)) | ((d[2].y ^ tag) | (d[2].w ^ tag) | (d[3].y ^ tag) | (d[3].w ^ tag));
}

__device__ __forceinline__ v4u ln_row(v4u in) {
  float v[8];
#pragma unroll
  for (int k = 0; k < 4; ++k) { v[2 * k] = lo16(in[k]); v[2 * k + 1] = hi16(in[k]); }
  const float m = xrow_sum(row16_sum(((v[0] + v[1]) + (v[2] + v[3])) + ((v[4] + v[5]) + (v[6] + v[7])))) * (1.f / 512.f);
  float q = 0.f;
#pragma unroll
  for (int j = 0; j < 8; ++j) { const float d = v[j] - m; q = fmaf(d, d, q); }
  const float var = xrow_sum(row16_sum(q)) * (1.f / 512.f);
  const float mr = rbf(m);
  const float den = rbf(__builtin_sqrtf(rbf(rbf(var) + 1e-5f)));
  const float inv = 1.f / den;
  v4u o;
#pragma unroll
  for (int k = 0; k < 4; ++k) {
    const unsigned d = pk(v[2 * k] - mr, v[2 * k + 1] - mr);
    o[k] = pk(lo16(d) * inv, hi16(d) * inv);
  }
  return o;
}

__device__ __forceinline__ void tr8(const v4u* in, v4u (&out)[8]) {
#pragma unroll
  for (int jp = 0; jp < 4; ++jp)
#pragma unroll
    for (int m = 0; m < 4; ++m) {
      out[2 * m][jp] = __builtin_amdgcn_perm(in[jp + 4][m], in[jp][m], 0x05040100u);
      out[2 * m + 1][jp] = __builtin_amdgcn_perm(in[jp + 4][m], in[jp][m], 0x07060302u);
    }
}

__device__ __forceinline__ bool all4(v4u f) { return __builtin_amdgcn_readfirstlane(f.x & f.y & f.z & f.w) != 0; }
template <int SLEEP> __device__ __forceinline__ void lds_wait4(LAS unsigned char* p) {
#pragma nounroll
  for (unsigned spins = 0; spins < SPIN_CAP; ++spins) {
    if (all4(*(volatile LAS v4u*)p)) break;
    if (SLEEP) __builtin_amdgcn_s_sleep(SLEEP);
  }
  CB();
}
template <int SLEEP> __device__ __forceinline__ void lds_wait_ge(LAS unsigned char* p, unsigned v) {
#pragma nounroll
  for (unsigned spins = 0; spins < SPIN_CAP; ++spins) {
    if ((unsigned)__builtin_amdgcn_readfirstlane(*(volatile LAS unsigned*)p) >= v) break;
    if (SLEEP) __builtin_amdgcn_s_sleep(SLEEP);
  }
  CB();
}
#define SETWORD(F, V) do { CB(); *(volatile LAS unsigned*)(L + (F) + w * 4) = (V); } while (0)
#define SETFLAG(F) SETWORD(F, 1u)

__global__ void __launch_bounds__(768, 3)
fused_kernel(const u16* __restrict__ x, const u16* __restrict__ kc, const u16* __restrict__ vc, const u16* __restrict__ wq,
             const u16* __restrict__ wo, const u16* __restrict__ w1, const u16* __restrict__ b1, const u16* __restrict__ w2,
             const u16* __restrict__ b2, u16* __restrict__ out, unsigned char* __restrict__ ws) {
  __shared__ __attribute__((aligned(16))) unsigned char smem[LDS_BYTES];
  LAS unsigned char* L = (LAS unsigned char*)smem;
  STAMP(0);
  const int tid = threadIdx.x, lane = tid & 63, wave = __builtin_amdgcn_readfirstlane(tid >> 6);
  const int wg = blockIdx.x, kvh = wg & 7, r = wg >> 3, b = r >> 3, s = r & 7;
  const int l16 = lane & 15, c = lane >> 4, dup = lane & 3, w = wave & 3;
  if (tid < 48) *(LAS unsigned*)(L + L_FLAG + tid * 4) = 0u;
  unsigned pf = 0;
  if (wave == PFW) {
    u64 pc; unsigned len;
    asm volatile("s_getpc_b64 %0\n.Lpc_here:\n\ts_mov_b32 %1, .Lk_end-.Lpc_here" : "=s"(pc), "=s"(len));
    pf = ld32<0>(mkrs((const void*)pc, len), r * 512 + lane * 8, 0);
  }
  const int ks = ((kvh + XMAP) & 7) * 8 + (r >> 2), ns = r & 3;
  const int ks2 = ((kvh + XMAP) & 7) * 7 + (r >> 2);
  const bool has2 = r < 28;
  const int qg = (kvh + XQ) & 7;
  const bool has1 = (r >> 2) != 3;
  const int u1 = kvh * 28 + (r < 12 ? r : r - 4);
  const int kq = r & 3, ke = u1 & 7;
  int tso[4];
#pragma unroll
  for (int t = 0; t < 4; ++t) tso[t] = (((b * 8 + kvh) * 4 + 3) * 128 + 32 * t + ((4 * s + w + ROT * t) & 31)) * 4096;
  const int koff = (l16 >> 2) * 256 + c * 64 + dup * 16;
  const int wqv = (lane >> 3) * 8192 + (lane & 7) * 16, wqs = (kq * 128 + w * 32) * 8192 + qg * 1024 + (r >> 2) * 128;
  const int w1v = (lane >> 3) * 3584 + (lane & 7) * 16, w1s = (ke * 64 + w * 16) * 3584 + (u1 >> 3) * 128;
  asm volatile("" :: "s"(tso[0]), "s"(tso[1]), "v"(koff), "v"(wqv), "s"(wqs), "v"(w1v), "s"(w1s));
  asm volatile("" :: "s"(x), "s"(kc), "s"(vc), "s"(wq), "s"(wo), "s"(w1), "s"(b1), "s"(w2), "s"(b2), "s"(out), "s"(ws));
  SB();
  asm volatile("s_waitcnt lgkmcnt(0)" ::: "memory");
  const unsigned ep = *(const CAS unsigned*)(ws + EP_OFF);
  SB();
#define BAR() do { SB(); CB(); __builtin_amdgcn_s_barrier(); CB(); SB(); } while (0)

  const rsrc_t rws = mkrs(ws, WS_BYTES);
  unsigned tag;
#define MAKE_TAG() asm volatile("s_add_u32 %0, %1, 1" : "=s"(tag) : "s"(ep) : "scc")

  v4u xr, wv[4], uv[2], K[16];
  const rsrc_t rk = mkrs(kc, (EXP & 16) ? 0 : 0x4000000);
  if (wave < 4) {
    const rsrc_t rwq = mkrs(wq, (EXP & 2) ? 0 : 512 * 8192);
    const rsrc_t rw1 = mkrs(w1, (has1 && !(EXP & 1)) ? 512 * 3584 : 0);
    xr = ld128<0>(mkrs(x, 4096), w * 1024 + lane * 16, 0);
#pragma unroll
    for (int n = 0; n < 4; ++n) wv[n] = ld128<AUX_NT>(rwq, wqv, wqs + n * 8 * 8192);
#pragma unroll
    for (int n = 0; n < 2; ++n) uv[n] = ld128<AUX_NT>(rw1, w1v, w1s + n * 8 * 3584);
    SB();
  } else if (wave < 8) {
    __builtin_amdgcn_s_sleep(KDELAY);
#pragma unroll
    for (int n = 0; n < WK; ++n) K[n] = ld128<AUX_NT>(rk, koff + (n & 3) * 1024, tso[n >> 2]);
    SB();
  }
  BAR();
  int wave2 = wave;
  asm volatile("" : "+s"(wave2));

  if (__builtin_expect(wave2 >= 8, 0)) {
    const rsrc_t rv = mkrs(vc, (EXP & 32) ? 0 : 0x4000000);
    const int voff = lane * 16;
    v4u V[16];
#pragma unroll
    for (int n = 0; n < 16; ++n) {
      if (n < WV) { if (n == 0 || n + 2 - (WV - WK) > 1) lds_wait_ge<1>(L + F_K + w * 4, n + 2 - (WV - WK) > 1 ? n + 2 - (WV - WK) : 1); }
      else WAITVM(WV - 1);
      V[n] = ld128<AUX_NT>(rv, voff + (n & 3) * 1024, tso[n >> 2]);
      SB();
    }
    MAKE_TAG();
    __builtin_amdgcn_s_setprio(3);
    STAMP(4);
    lds_wait4<1>(L + F_P);
    v4u P[2];
    P[0] = *(LAS v4u*)(L + L_P + w * 512 + (l16 >> 2) * 64 + c * 16);
    P[1] = *(LAS v4u*)(L + L_P + w * 512 + 256 + (l16 >> 2) * 64 + c * 16);
    float wgt[4];
    unsigned lse2;
    {
      const f32x4 s0 = *(LAS f32x4*)(L + L_ST + w * 32), s1 = *(LAS f32x4*)(L + L_ST + w * 32 + 16);
      const float M = fmaxf(fmaxf(s0[0], s0[2]), fmaxf(s1[0], s1[2]));
      wgt[0] = ex2((s0[0] - M) * LOG2E);
      wgt[1] = ex2((s0[2] - M) * LOG2E);
      wgt[2] = ex2((s1[0] - M) * LOG2E);
      wgt[3] = ex2((s1[2] - M) * LOG2E);
      const float Ls = wgt[0] * s0[1] + wgt[1] * s0[3] + wgt[2] * s1[1] + wgt[3] * s1[3];
      const float inv = rcp(Ls);
#pragma unroll
      for (int k = 0; k < 4; ++k) wgt[k] *= inv;
      lse2 = __float_as_uint(M * LOG2E + __builtin_amdgcn_logf(Ls));
    }
    const int ksp = (kvh * 4 + w) * 2 + (c & 1);
    const int p1off = lane < 32 ? P1_OFF + (ksp * 32 + ((r + SKEW * ksp) & 31)) * 256 + l16 * 16
                    : lane < 40 ? L1_OFF + (((kvh * 4 + w) * 4 + b) * 8 + s) * 128 + l16 * 16 : OOB;
    const int ppw = L_PART + w * 2048 + c * 512 + l16 * 32, ppr = L_PART + w * 512 + (lane & 31) * 16;
    f32x4 o[8];
    {
      v4u vt[8];
      tr8(&V[0], vt);
#pragma unroll
      for (int e = 0; e < 8; ++e) o[e] = mfma(P[0], vt[e], f32x4{0.f, 0.f, 0.f, 0.f});
    }
    asm volatile("" :: "v"(p1off), "v"(ppw), "v"(ppr), "v"(wgt[0]), "v"(wgt[1]), "v"(wgt[2]), "v"(wgt[3]), "v"(lse2));
    SB();
    {
      v4u vt[8];
#pragma unroll
      for (int jp = 0; jp < 4; ++jp) {
        if (jp == 3 - WOAT) { WAITVM(WOAT); SETFLAG(F_N); }
#pragma unroll
        for (int m = 0; m < 4; ++m) {
          vt[2 * m][jp] = __builtin_amdgcn_perm(V[12 + jp][m], V[8 + jp][m], 0x05040100u);
          vt[2 * m + 1][jp] = __builtin_amdgcn_perm(V[12 + jp][m], V[8 + jp][m], 0x07060302u);
        }
        if (jp < 3) SB();
      }
#pragma unroll
      for (int e = 0; e < 8; ++e) o[e] = mfma(P[1], vt[e], o[e]);
    }
    ((LAS f32x4*)(L + ppw))[0] = f32x4{o[0][0], o[1][0], o[2][0], o[3][0]};
    ((LAS f32x4*)(L + ppw))[1] = f32x4{o[4][0], o[5][0], o[6][0], o[7][0]};
    SETFLAG(F_O);
    STAMP(11);
    {
      f32x4 r0, r1, r2, r3;
#pragma nounroll
      for (unsigned spins = 0; spins < SPIN_CAP; ++spins) {
        const v4u f = *(volatile LAS v4u*)(L + F_O);
        r0 = *(volatile LAS f32x4*)(L + ppr); r1 = *(volatile LAS f32x4*)(L + ppr + 2048);
        r2 = *(volatile LAS f32x4*)(L + ppr + 4096); r3 = *(volatile LAS f32x4*)(L + ppr + 6144);
        if (all4(f)) break;
      }
      const f32x4 a = (wgt[0] * r0 + wgt[1] * r1) + (wgt[2] * r2 + wgt[3] * r3);
      st128(rws, p1off, v4u{lane < 32 ? pk(a[0], a[1]) : lse2, tag, pk(a[2], a[3]), tag});
    }
    STAMP(5);
    asm volatile(".Lk_end:");
    DUMP_STAMPS();
    return;
  }

  if (wave2 >= 4) {
    const unsigned t0 = (unsigned)__builtin_amdgcn_s_memrealtime();
    SB();
#pragma unroll
    for (int n = WK; n < 16; ++n) {
      WAITVM(WK - 1);
      K[n] = ld128<AUX_NT>(rk, koff + (n & 3) * 1024, tso[n >> 2]);
      SB();
    }
    STAMP(1);
    SETWORD(F_K, 1u);
#pragma unroll
    for (int j = 1; j <= WK; ++j) {
      WAITVM(WK - j);
      SETWORD(F_K, 1u + j);
    }
    SB();
    const unsigned tk = (unsigned)__builtin_amdgcn_s_memrealtime();
    const unsigned tlim = ((tk - t0) * RN) >> 4;
    MAKE_TAG();
    asm volatile("" :: "v"(pf));
    STAMP(2);
    lds_wait4<1>(L + F_Q);
    STAMP(13);
    const v4u qf = *(LAS v4u*)(L + L_Q + dup * 256 + c * 64 + (l16 >> 2) * 16);
    float sc[4];
#pragma unroll
    for (int mm = 0; mm < 4; ++mm) {
      float t[4];
#pragma unroll
      for (int be = 0; be < 4; ++be) {
        const f32x4 d = mfma(K[4 * mm + be], qf, f32x4{0.f, 0.f, 0.f, 0.f});
        t[be] = ((d[be] + dpp<0x12C>(d[(be + 1) & 3])) + dpp<0x128>(d[(be + 2) & 3])) + dpp<0x124>(d[(be + 3) & 3]);
      }
      const float t01 = (l16 & 4) ? t[1] : t[0], t23 = (l16 & 4) ? t[3] : t[2];
      sc[mm] = (l16 & 8) ? t23 : t01;
    }
    {
      const unsigned d0 = pk(sc[0], sc[1]), d1 = pk(sc[2], sc[3]);
      const unsigned e0 = pk(lo16(d0) * 0.08838834764831845f, hi16(d0) * 0.08838834764831845f);
      const unsigned e1 = pk(lo16(d1) * 0.08838834764831845f, hi16(d1) * 0.08838834764831845f);
      sc[0] = lo16(e0); sc[1] = hi16(e0); sc[2] = lo16(e1); sc[3] = hi16(e1);
    }
    float m = fmaxf(fmaxf(sc[0], sc[1]), fmaxf(sc[2], sc[3]));
    m = fmaxf(m, dpp<0x124>(m)); m = fmaxf(m, dpp<0x128>(m));
    m = xrow_max(m);
    const float p0 = ex2((sc[0] - m) * LOG2E), p1 = ex2((sc[1] - m) * LOG2E), p2 = ex2((sc[2] - m) * LOG2E), p3 = ex2((sc[3] - m) * LOG2E);
    *(LAS unsigned*)(L + L_P + w * 512 + dup * 64 + c * 16 + (l16 >> 2) * 4) = pk(p0, p1);
    *(LAS unsigned*)(L + L_P + w * 512 + 256 + dup * 64 + c * 16 + (l16 >> 2) * 4) = pk(p2, p3);
    float l = (p0 + p1) + (p2 + p3);
    l += dpp<0x124>(l); l += dpp<0x128>(l);
    l = xrow_sum(l);
    if (lane < 4) *(LAS f32x2*)(L + L_ST + lane * 32 + w * 8) = f32x2{m, l};
    SETFLAG(F_P);
    STAMP(3);
    const rsrc_t rwo = mkrs(wo, (EXP & 4) ? 0 : 4096 * 1024);
    const rsrc_t rw2 = mkrs(w2, (has2 && !(EXP & 4)) ? 1792 * 1024 : 0);
    const int wov = c * 1024 + l16 * 16;
    const int fpair = (ns * 2 + (w >> 1)) + 8 * ((l16 >> 3) * 8 + (w & 1) * 4 + c);
    const int sk = ks + SKEW * (fpair >> 3), sk2 = ks2 + SKEW * (fpair >> 3);
    const int p2off = P2_OFF + (fpair * 64 + (sk & 63)) * 128 + (l16 & 7) * 16;
    const int g2off = has2 ? G2_OFF + (fpair * 64 + (sk2 >= 56 ? sk2 - 56 : sk2)) * 128 + (l16 & 7) * 16 : OOB;
    const int osr = L_OS + ((l16 >> 2) * 8 + dup) * OS_STRIDE + c * 16;
    const int osl = L_OS + ((l16 >> 2) * 8 + dup) * OS_STRIDE + 128;
    asm volatile("" :: "v"(p2off), "v"(g2off), "v"(osr), "v"(osl), "v"(wov));
#pragma nounroll
    for (unsigned spins = 0; spins < SPIN_CAP; ++spins) {
      if (__builtin_amdgcn_readfirstlane(*(volatile LAS unsigned*)(L + F_N + w * 4)) != 0) break;
      if (XT ? (unsigned)__builtin_amdgcn_s_memrealtime() - t0 > XT : (unsigned)__builtin_amdgcn_s_memrealtime() - tk > tlim) break;
      __builtin_amdgcn_s_sleep(1);
    }
    CB();
    v4u WO[6];
#pragma unroll
    for (int n = 0; n < 4; ++n) WO[n] = ld128<AUX_NT>(rwo, wov, (ks * 64 + 16 * w + 4 * n) * 1024 + ns * 256);
#pragma unroll
    for (int n = 0; n < 2; ++n) WO[4 + n] = ld128<AUX_NT>(rw2, wov, (ks2 * 32 + 8 * w + 4 * n) * 1024 + ns * 256);
    SB();
    SETFLAG(F_T);
    STAMP(4);
#pragma unroll
    for (int n = 0; n < 4; ++n) *(LAS v4u*)(L + L_WO + swap23(16 * w + 4 * n + c) * WO_RS + l16 * 16) = WO[n];
#pragma unroll
    for (int n = 0; n < 2; ++n) *(LAS v4u*)(L + L_WO + (64 + swap23(8 * w + 4 * n + c)) * WO_RS + l16 * 16) = WO[4 + n];
    SETFLAG(F_WO);
    STAMP(14);
    lds_wait4<0>(L + F_WO);
    v4u af[2][2];
    {
      LAS unsigned char* tw = L + L_WO + ((c >> 1) * 16 + (c & 1) * 4 + (l16 >> 2)) * WO_RS + w * 64 + dup * 8;
#pragma unroll
      for (int ct = 0; ct < 2; ++ct)
#pragma unroll
        for (int kk = 0; kk < 2; ++kk) {
          const v2u a0 = trread(tw + ct * 32 + kk * 32 * WO_RS), a1 = trread(tw + ct * 32 + kk * 32 * WO_RS + 8 * WO_RS);
          af[ct][kk] = v4u{a0.x, a0.y, a1.x, a1.y};
        }
      lds_wait_ge<1>(L + F_H, 1u);
      const v4u hb = *(LAS v4u*)(L + L_H + (l16 >> 2) * 64 + c * 16);
      f32x4 g[2];
#pragma unroll
      for (int ct = 0; ct < 2; ++ct) {
        const v2u a0 = trread(tw + ct * 32 + 64 * WO_RS), a1 = trread(tw + ct * 32 + 72 * WO_RS);
        g[ct] = mfma(v4u{a0.x, a0.y, a1.x, a1.y}, hb, f32x4{0.f, 0.f, 0.f, 0.f});
      }
      const f32x4 acc = (dup & 2) ? g[1] : g[0];
      const float v0 = (dup & 1) ? acc[2] : acc[0], v1 = (dup & 1) ? acc[3] : acc[1];
      st128(rws, g2off, v4u{__float_as_uint(v0), tag, __float_as_uint(v1), tag});
    }
    asm volatile("" :: "v"(af[0][0]), "v"(af[0][1]), "v"(af[1][0]), "v"(af[1][1]));
    STAMP(6);
    {
      v4u bf[2][2];
      float ll, lh;
#pragma nounroll
      for (unsigned spins = 0; spins < SPIN_CAP; ++spins) {
        const v4u f = *(volatile LAS v4u*)(L + F_OS);
        bf[0][0] = *(volatile LAS v4u*)(L + osr); bf[0][1] = *(volatile LAS v4u*)(L + osr + 64);
        bf[1][0] = *(volatile LAS v4u*)(L + osr + 4 * OS_STRIDE); bf[1][1] = *(volatile LAS v4u*)(L + osr + 4 * OS_STRIDE + 64);
        ll = *(volatile LAS float*)(L + osl); lh = *(volatile LAS float*)(L + osl + 4 * OS_STRIDE);
        if (all4(f)) break;
      }
      f32x4 acc[2][2];
#pragma unroll
      for (int h = 0; h < 2; ++h)
#pragma unroll
        for (int ct = 0; ct < 2; ++ct) acc[ct][h] = mfma(af[ct][1], bf[h][1], mfma(af[ct][0], bf[h][0], f32x4{0.f, 0.f, 0.f, 0.f}));
      float M = fmaxf(ll, lh);
      M = fmaxf(M, dpp<0xB1>(M)); M = fmaxf(M, dpp<0x4E>(M));
      float el = ex2(ll - M), eh = ex2(lh - M);
      float den = el + eh;
      den += dpp<0xB1>(den); den += dpp<0x4E>(den);
      const float inv = rcp(den);
      el *= inv; eh *= inv;
      const f32x4 v0 = el * acc[0][0] + eh * acc[0][1], v1 = el * acc[1][0] + eh * acc[1][1];
      const f32x4 keep = (dup & 2) ? v1 : v0, send = (dup & 2) ? v0 : v1;
      float t[4];
#pragma unroll
      for (int i = 0; i < 4; ++i) t[i] = keep[i] + dpp<0x4E>(send[i]);
      const float k0 = (dup & 1) ? t[2] : t[0], k1 = (dup & 1) ? t[3] : t[1], s0 = (dup & 1) ? t[0] : t[2], s1 = (dup & 1) ? t[1] : t[3];
      const float r0 = k0 + dpp<0xB1>(s0), r1 = k1 + dpp<0xB1>(s1);
      st128(rws, p2off, v4u{__float_as_uint(r0), tag, __float_as_uint(r1), tag});
    }
    STAMP(7);
  } else {
    __builtin_amdgcn_s_setprio(2);
    const v4u xn = ln_row(xr);
    *(LAS v4u*)(L + L_XN + w * XS_STRIDE + lane * 16) = xn;
    MAKE_TAG();
    STAMP(1);
    const int sw = (((lane >> 5) & 1) * 8 + ((lane >> 3) & 3)) * WT_RS + (lane & 7) * 16;
#pragma unroll
    for (int n = 0; n < 4; ++n) *(LAS v4u*)(L + L_WQ + sw + (w * 32 + (n >> 1) * 16 + (n & 1) * 4) * WT_RS) = wv[n];
    SETFLAG(F_A);
    const int ta = ((c >> 1) * 16 + (c & 1) * 4 + (l16 >> 2)) * WT_RS + w * 32 + dup * 8;
    const int xb = (l16 >> 2) * XS_STRIDE + c * 16;
    lds_wait4<0>(L + F_A);
    {
      f32x4 acc = {0.f, 0.f, 0.f, 0.f};
#pragma unroll
      for (int kk = 0; kk < 4; ++kk) {
        const v2u a0 = trread(L + L_WQ + ta + kk * 32 * WT_RS), a1 = trread(L + L_WQ + ta + (kk * 32 + 8) * WT_RS);
        acc = mfma(*(LAS v4u*)(L + L_XN + xb + kq * 256 + kk * 64), v4u{a0.x, a0.y, a1.x, a1.y}, acc);
      }
      st64(rws, QP_OFF + ((((qg * 4 + c) * 4 + (r >> 3)) * 4 + kq) * 128 + ((r >> 2) & 1) * 64 + w * 16 + l16) * 8, v2u{__float_as_uint(acc[0]), tag});
    }
    SB();
    STAMP(2);
    const v4u yn = ln_row(xn);
    *(LAS v4u*)(L + L_YN + w * XS_STRIDE + lane * 16) = yn;
#pragma unroll
    for (int n = 0; n < 2; ++n) *(LAS v4u*)(L + L_W1 + sw + (w * 16 + n * 4) * WT_RS) = uv[n];
    SETFLAG(F_B);
    lds_wait4<0>(L + F_B);
    {
      f32x4 acc = {0.f, 0.f, 0.f, 0.f};
#pragma unroll
      for (int kk = 0; kk < 2; ++kk) {
        const v2u a0 = trread(L + L_W1 + ta + kk * 32 * WT_RS), a1 = trread(L + L_W1 + ta + (kk * 32 + 8) * WT_RS);
        acc = mfma(*(LAS v4u*)(L + L_YN + xb + ke * 128 + kk * 64), v4u{a0.x, a0.y, a1.x, a1.y}, acc);
      }
      const int off = ZP_OFF + (((((u1 >> 3) * 2 + (w >> 1)) * 8 + ke) * 4 + c) * 32 + (w & 1) * 16 + l16) * 8;
      st64(rws, has1 ? off : OOB, v2u{__float_as_uint(acc[0]), tag});
    }
    STAMP(3);
    const int oidx = (wg >> 6) * 512 + kvh * 64 + ((wg >> 5) & 1) * 32 + ((wg >> 3) & 3) * 4 + ((lane >> 1) & 1) * 16 + (lane & 1) * 2;
    const unsigned xw = ld32<0>(mkrs(x, 4096), oidx * 2, 0);
    const unsigned bw = ld32<0>(mkrs(b2, 1024), (oidx & 511) * 2, 0);
    const unsigned bias = ld32<0>(mkrs(b1, 3584), (ks2 * 32 + l16 * 2) * 2, 0);
    SB();
    __builtin_amdgcn_s_sleep(QDELAY);
    {
      v4u qd[4] = {};
      if (!(EXP & 8)) GATHER4(1024, rws, QP_OFF + ((kvh * 4 + b) * 4 + w) * 4096 + lane * 16, 2048, tag, ~0ull, qd);
      *(LAS unsigned*)(L + L_Q + w * 256 + lane * 4) = pk(sumx(qd), sumz(qd));
      SETFLAG(F_Q);
    }
    STAMP(4);
    if (wave2 == 3) {
      float z0 = 0.f, z1 = 0.f;
      if (has2 && !(EXP & 64)) {
        const int zoff = ZP_OFF + ks2 * 8192 + lane * 16;
        v4u zd[4] = {}, ze[4];
#pragma unroll
        for (int k = 0; k < 4; ++k) ze[k] = ld128<AUX_SC1>(rws, zoff + k * 1024, 4096);
        SB();
        GATHER4(1024, rws, zoff, 2048, tag, ~0ull, zd);
        z0 = sumx(zd); z1 = sumz(zd);
        asm volatile("" : "+v"(z0), "+v"(z1));
        const u64 miss = __builtin_amdgcn_ballot_w64(tagdiff(ze, tag) != 0);
        if (miss) GATHER4(1024, rws, zoff + 4096, 2048, tag, miss, ze);
        z0 += sumx(ze); z1 += sumz(ze);
      }
      const unsigned zp = pk(z0, z1);
      const unsigned zb = pk(lo16(zp) + lo16(bias), hi16(zp) + hi16(bias));
      float z[2] = {lo16(zb), hi16(zb)};
#pragma unroll
      for (int i = 0; i < 2; ++i) {
        const float zz = z[i];
        const float u = 0.7978845608028654f * (zz + 0.044715f * zz * zz * zz);
        const float th = 1.f - 2.f * rcp(ex2(u * (2.f * LOG2E)) + 1.f);
        z[i] = 0.5f * zz * (1.f + th);
      }
      *(LAS unsigned*)(L + L_H + lane * 4) = pk(z[0], z[1]);
      CB();
      *(volatile LAS unsigned*)(L + F_H) = 1u;
      STAMP(5);
    }
    const int sp = lane & 7, k4 = lane >> 3;
    const int osw = L_OS + (w * 8 + sp) * OS_STRIDE + k4 * 8;
    const int p1g = P1_OFF + (ks * 32 + ((w * 8 + sp + SKEW * ks) & 31)) * 256 + k4 * 16;
    const int l1g = L1_OFF + (((ks >> 1) * 4 + w) * 8 + sp) * 128;
    const int foff = ((wg >> 7) * 64 + (wg & 63)) * 8192 + ((wg >> 6) & 1) * 64 + (lane >> 2) * 128 + dup * 16;
    const int fos = F_OS + w * 4, osl = L_OS + (w * 8 + sp) * OS_STRIDE + 128;
    unsigned one = 1u;
    asm volatile("" : "+v"(one) : "v"(osw), "v"(l1g), "v"(p1g), "v"(xw), "v"(bw), "v"(foff), "v"(fos), "v"(osl));
    lds_wait4<2>(L + F_T);
    __builtin_amdgcn_s_sleep(PSL);
    STAMP(12);
    {
      v4u d[3];
      GATHER3(rws, p1g, l1g, tag, ~0ull, d);
      STAMP(6);
      ((LAS unsigned*)(L + osw))[0] = d[0].x; ((LAS unsigned*)(L + osw))[1] = d[0].z;
      ((LAS unsigned*)(L + osw))[16] = d[1].x; ((LAS unsigned*)(L + osw))[17] = d[1].z;
      *(LAS unsigned*)(L + osl) = d[2].x;
      CB();
      *(volatile LAS unsigned*)(L + fos) = one;
    }
    if (wave2 == 0) {
      v4u dg[4] = {};
      if (GSL) __builtin_amdgcn_s_sleep(GSL);
      GATHER4(1792, rws, G2_OFF + foff, 3584, tag, (1ull << 56) - 1ull, dg);
      float g0 = sumx(dg), g1 = sumz(dg);
      asm volatile("" : "+v"(g0), "+v"(g1));
      STAMP(8);
      if (FSL) __builtin_amdgcn_s_sleep(FSL);
      v4u ds[4];
#pragma unroll
      for (int k = 0; k < 4; ++k) ds[k] = ld128<AUX_SC1>(rws, P2_OFF + foff + (k & 1) * 2048, (k >> 1) * 4096);
      SB();
      g0 += dpp<0x124>(g0); g1 += dpp<0x124>(g1);
      g0 += dpp<0x128>(g0); g1 += dpp<0x128>(g1);
      g0 = xrow_sum(g0); g1 = xrow_sum(g1);
      const unsigned gp = pk(g0, g1);
      const unsigned gb = pk(lo16(gp) + lo16(bw), hi16(gp) + hi16(bw));
      const float z0 = lo16(gb), z1 = hi16(gb);
      const unsigned sg = pk(z0 * rcp(1.f + ex2(-z0 * LOG2E)), z1 * rcp(1.f + ex2(-z1 * LOG2E)));
      float sg0 = lo16(sg), sg1 = hi16(sg), x0 = lo16(xw), x1 = hi16(xw);
      GAS unsigned* op = (GAS unsigned*)(out + oidx);
      int epo = (wg == 0 && lane < 8) ? EP_OFF + lane * 16 : OOB;
      asm volatile("" : "+v"(sg0), "+v"(sg1), "+v"(x0), "+v"(x1), "+v"(op), "+v"(epo));
      SB();
      const u64 miss = __builtin_amdgcn_ballot_w64(tagdiff(ds, tag) != 0);
      if (miss) GATHER4(2048, rws, P2_OFF + foff, 4096, tag, miss, ds);
      STAMP(9);
      float s0 = sumx(ds), s1 = sumz(ds);
      s0 += dpp<0x124>(s0); s1 += dpp<0x124>(s1);
      s0 += dpp<0x128>(s0); s1 += dpp<0x128>(s1);
      s0 = xrow_sum(s0); s1 = xrow_sum(s1);
      const unsigned sq = pk(s0, s1);
      const unsigned pr = pk(lo16(sq) * sg0, hi16(sq) * sg1);
      const unsigned res = pk(x0 + lo16(pr), x1 + hi16(pr));
      if (lane < 4) *op = res;
      st128(rws, epo, v4u{tag, tag, tag, tag});
      STAMP(10);
    }
  }
  DUMP_STAMPS();
}

extern "C" void kernel_launch(void* const* d_in, const int* in_sizes, int n_in,
                              void* d_out, int out_size, void* d_ws, size_t ws_size,
                              hipStream_t stream) {
  fused_kernel<<<256, 768, 0, stream>>>((const u16*)d_in[0], (const u16*)d_in[1], (const u16*)d_in[2], (const u16*)d_in[3],
                                        (const u16*)d_in[4], (const u16*)d_in[6], (const u16*)d_in[7], (const u16*)d_in[8],
                                        (const u16*)d_in[9], (u16*)d_out, (unsigned char*)d_ws);
}
```
